```python
import torch

def _softmax(x, axis=-1):
    return torch.softmax(x, dim=axis)

def _gelu(x):
    return torch.nn.functional.gelu(x, approximate='tanh')

def _silu(x):
    return torch.nn.functional.silu(x)

def _sigmoid(x):
    return torch.sigmoid(x)

def _layernorm(x, axis=-1, eps=1e-05):
    m = x.mean(dim=axis, keepdim=True)
    v = x.var(dim=axis, keepdim=True, unbiased=False)
    return (x - m) / torch.sqrt(v + eps)

def _scatter_add(tgt, idx, src, axis=0):
    return tgt.clone().index_add_(axis, idx, src)

def _segment_sum(src, seg, n):
    out = torch.zeros((n,) + src.shape[1:], dtype=src.dtype)
    return out.index_add_(0, seg, src)

def _pad(x, pw):
    flat = tuple((v for pair in reversed(pw) for v in pair))
    return torch.nn.functional.pad(x, flat)
kernel_name = 'sequential_preLN__ssm_conv_s4d__mixer_gmlp__prompt'

def setup_inputs():

    _w = lambda a: (lambda t: t.to(torch.bfloat16) if t.is_floating_point() else t)(torch.as_tensor(a))

    def _A_setup():
        Bsz, L, D, N = (1, 2048, 640, 128)
        return {'u': _w((lambda *s: torch.randn(*s, dtype=torch.bfloat16) * 0.05)(Bsz, L, D)), 'log_A_real': _w((lambda *s: torch.randn(*s, dtype=torch.bfloat16) * 6.4)(D, N) * 0.5), 'A_imag': _w((lambda *s: torch.randn(*s, dtype=torch.bfloat16) * 0.05)(D, N) * 3.14), 'C_real': _w((lambda *s: torch.randn(*s, dtype=torch.bfloat16) * 0.05)(D, N) / N ** 0.5), 'C_imag': _w((lambda *s: torch.randn(*s, dtype=torch.bfloat16) * 0.05)(D, N) / N ** 0.5), 'log_dt': _w((lambda *s: torch.randn(*s, dtype=torch.bfloat16) * 0.05)(D) * 0.5 - 4.0), 'D_skip': _w((lambda *s: torch.randn(*s, dtype=torch.bfloat16) * 0.05)(D))}
    _A_in = _A_setup()

    def _B_setup():
        B, S, D, F = (1, 2048, 640, 1728)
        Fh = F // 2
        return {'x': _w((lambda *s: torch.randn(*s, dtype=torch.bfloat16) * 0.05)(B, S, D)), 'w_in': _w((lambda *s: torch.randn(*s, dtype=torch.bfloat16) * 0.4)(D, 2 * Fh)), 'w_sp': _w(0.01 * (lambda *s: torch.randn(*s, dtype=torch.bfloat16) * 0.4)(S, S)), 'b_sp': _w(1.0 + 0.01 * (lambda *s: torch.randn(*s, dtype=torch.bfloat16) * 0.05)(S)), 'w_out': _w((lambda *s: torch.randn(*s, dtype=torch.bfloat16) * 0.05)(Fh, D))}
    _B_in = _B_setup()
    return {'A_u': _A_in['u'], 'A_log_A_real': _A_in['log_A_real'], 'A_A_imag': _A_in['A_imag'], 'A_C_real': _A_in['C_real'], 'A_C_imag': _A_in['C_imag'], 'A_log_dt': _A_in['log_dt'], 'A_D_skip': _A_in['D_skip'], 'B_x': _B_in['x'], 'B_w_in': _B_in['w_in'], 'B_w_sp': _B_in['w_sp'], 'B_b_sp': _B_in['b_sp'], 'B_w_out': _B_in['w_out']}

def reference(A_u, A_log_A_real, A_A_imag, A_C_real, A_C_imag, A_log_dt, A_D_skip, B_x, B_w_in, B_w_sp, B_b_sp, B_w_out):

    def _A_ref(u, log_A_real, A_imag, C_real, C_imag, log_dt, D_skip):
        L = u.shape[1]
        dt = torch.exp(log_dt)
        A = -torch.exp(log_A_real) + 1j * A_imag
        C = (C_real + 1j * C_imag) * dt[:, None]
        pos = (lambda x, dt: x.to(dt))(torch.arange(L), log_dt.dtype)
        P = torch.exp((A * dt[:, None])[:, :, None] * pos[None, None, :])
        K = 2.0 * torch.einsum('dn,dnl->dl', C, P).real
        L2 = 2 * L
        u_t = (lambda x, *d: x.permute(*d))(u, 0, 2, 1)
        Kf = (lambda x, *a, **k: torch.fft.rfft(x.float(), *a, **k))(K, L2)
        Uf = (lambda x, *a, **k: torch.fft.rfft(x.float(), *a, **k))(u_t, L2)
        y = (lambda x, *a, **k: torch.fft.irfft(x, *a, **k).to(torch.bfloat16))(Uf * Kf[None, :, :], L2)[:, :, :L]
        y = (lambda x, *d: x.permute(*d))(y, 0, 2, 1)
        return y + u * D_skip[None, None, :]

    def _B_ref(x, w_in, w_sp, b_sp, w_out):
        B, S, D = x.shape
        F = w_in.shape[-1]
        Fh = F // 2
        z = _gelu(torch.einsum('bsd,df->bsf', x, w_in))
        z1 = z[..., :Fh].clone()
        z2 = _layernorm(z[..., Fh:], axis=-1)
        g = torch.einsum('ts,bsf->btf', w_sp, z2) + b_sp[None, :, None]
        return torch.einsum('bsf,fd->bsd', z1 * g, w_out)
    x = A_u
    h = x + _A_ref(_layernorm(x, axis=-1), A_log_A_real, A_A_imag, A_C_real, A_C_imag, A_log_dt, A_D_skip)
    h2 = h + _B_ref(_layernorm(h, axis=-1), B_w_in, B_w_sp, B_b_sp, B_w_out)
    return h2
```

```cpp
#include <hip/hip_runtime.h>
#include <hip/hip_bf16.h>
#include <stdint.h>

typedef float f2 __attribute__((ext_vector_type(2)));
typedef float f4 __attribute__((ext_vector_type(4)));
typedef float f16v __attribute__((ext_vector_type(16)));
typedef _Float16 h2 __attribute__((ext_vector_type(2)));
typedef _Float16 h4 __attribute__((ext_vector_type(4)));
typedef _Float16 h8 __attribute__((ext_vector_type(8)));
typedef __bf16 b2 __attribute__((ext_vector_type(2)));
typedef __bf16 b8 __attribute__((ext_vector_type(8)));
typedef short s4 __attribute__((ext_vector_type(4)));
typedef unsigned u2 __attribute__((ext_vector_type(2)));
typedef unsigned u4 __attribute__((ext_vector_type(4)));
typedef unsigned short us;
#define LAS __attribute__((address_space(3)))
#define GAS __attribute__((address_space(1)))
#define RLX_AGENT __ATOMIC_RELAXED, __HIP_MEMORY_SCOPE_AGENT

#define STAMP_DECL
#define STAMP(i)
#define STAMP_OUT(ws, kid)

constexpr int L_ = 2048, D_ = 640, NS = 128, F_ = 1728, FH = 864;

constexpr size_t WS_EPOCH = 0;
constexpr size_t WS_G1    = 4096;
constexpr size_t WS_CS    = WS_G1 + 16384;
constexpr size_t WS_CLR   = WS_CS + 8192;
constexpr size_t WS_G2    = WS_CLR;
constexpr size_t WS_ZS    = WS_G2 + 16384;
constexpr size_t WS_FL    = WS_ZS + 262144;
constexpr size_t WS_CLR_END = WS_FL + 16384;
constexpr size_t WS_HT    = 1 << 20;
constexpr size_t WS_Z1    = 4 << 20;
constexpr size_t WS_Z2    = 8 << 20;
constexpr size_t WS_P     = 12 << 20;
constexpr size_t WS_TMP   = 16 << 20;

__device__ __forceinline__ float bf2f(unsigned v16) { return __uint_as_float(v16 << 16); }
__device__ __forceinline__ float bflo(unsigned v) { return __uint_as_float(v << 16); }
__device__ __forceinline__ float bfhi(unsigned v) { return __uint_as_float(v & 0xffff0000u); }
__device__ __forceinline__ unsigned pkbf(float a, float b) { f2 v = {a, b}; b2 r = __builtin_convertvector(v, b2); return __builtin_bit_cast(unsigned, r); }
__device__ __forceinline__ unsigned pkh(float a, float b) { f2 v = {a, b}; h2 r = __builtin_convertvector(v, h2); return __builtin_bit_cast(unsigned, r); }
__device__ __forceinline__ float rbf(float a) { return bflo(pkbf(a, a)); }
__device__ __forceinline__ f2 cmul(f2 x, f2 y) { f2 t = x * y.xx; f2 ys = {-y.y, y.y}; return __builtin_elementwise_fma(x.yx, ys, t); }
__device__ __forceinline__ f2 csq(f2 x) { f2 r; r.x = x.x * x.x - x.y * x.y; r.y = 2.f * x.x * x.y; return r; }
__device__ __forceinline__ f2 cexpk(float ar2, float rev, float p) {
  float m = __builtin_amdgcn_exp2f(ar2 * p); float f = __builtin_amdgcn_fractf(rev * p);
  f2 r = {m * __builtin_amdgcn_cosf(f), m * __builtin_amdgcn_sinf(f)}; return r; }
template <int CTRL> __device__ __forceinline__ float dppf(float v) { return __builtin_bit_cast(float, __builtin_amdgcn_update_dpp(0, __builtin_bit_cast(int, v), CTRL, 0xf, 0xf, true)); }
__device__ __forceinline__ float rowsum16(float v) {
  v += dppf<0x128>(v); v += dppf<0x124>(v); v += dppf<0x122>(v); v += dppf<0x121>(v); return v; }

constexpr int A_CS = 4160;
constexpr int A_TOEP = 4 * A_CS;
constexpr int A_TAB = 3 * A_TOEP;
constexpr int A_ROW = 528;
constexpr int A_TABCH = 64 * A_ROW;
constexpr int A_UREV = A_TAB;
constexpr int A_RT = A_UREV + 3 * 4608;
constexpr int A_EX = A_RT + 3 * 8192;
constexpr int A_MISC = A_TAB + 3 * A_TABCH;
constexpr int A_LDS = A_MISC + 64;

__global__ __launch_bounds__(512, 2) void kA(const us* __restrict__ x, const us* __restrict__ lar, const us* __restrict__ aim,
                                             const us* __restrict__ cre, const us* __restrict__ cim, const us* __restrict__ ldt,
                                             const us* __restrict__ dsk, const us* __restrict__ win, char* __restrict__ ws) {
  extern __shared__ __attribute__((aligned(16))) char lds[];
  LAS char* S = (LAS char*)lds;
  const int tid = threadIdx.x, lane = tid & 63, w = __builtin_amdgcn_readfirstlane(tid >> 6), bid = blockIdx.x;
  const int slot = bid >> 3, cb = (bid & 7) * 27 + slot;
  const bool conv = slot < 27 && cb < 214;
  const int d0 = min(3 * cb, 637), cfirst = conv ? 3 * cb - d0 : 3;
  GAS unsigned long long* g1 = (GAS unsigned long long*)(ws + WS_G1);
  STAMP_DECL STAMP(0);
  if (w >= 6) {
    const int row = 8 * bid + 4 * (w - 6) + (lane >> 4), c = lane & 15;
    const u4* xr = (const u4*)(x + (size_t)row * D_);
    u4 v[5];
#pragma unroll
    for (int i = 0; i < 5; ++i) v[i] = xr[c + 16 * i];
    const unsigned tag = __hip_atomic_load((GAS unsigned*)(ws + WS_EPOCH), RLX_AGENT) + 1u;
    float s = 0.f;
#pragma unroll
    for (int i = 0; i < 5; ++i)
#pragma unroll
      for (int k = 0; k < 4; ++k) s += bflo(v[i][k]) + bfhi(v[i][k]);
    const float mean = rowsum16(s) * (1.f / D_);
    float q = 0.f;
#pragma unroll
    for (int i = 0; i < 5; ++i)
#pragma unroll
      for (int k = 0; k < 4; ++k) { float a = bflo(v[i][k]) - mean, b = bfhi(v[i][k]) - mean; q += a * a + b * b; }
    const float var = rbf(rowsum16(q) * (1.f / D_));
    const float den = __builtin_sqrtf(rbf(var + 1e-5f));
    if (c == 0) __hip_atomic_store(g1 + row, ((unsigned long long)tag << 32) | pkbf(mean, den), RLX_AGENT);
    if (tid == 6 * 64) *(LAS unsigned*)(S + A_MISC) = tag;
  }
  STAMP(1);
  if (!conv) {
    const int sj = slot >= 27 ? (slot - 27) * 8 + (bid & 7) : 40 + (cb - 214);
    { const int i = sj * 512 + tid; if (i < (int)((WS_CLR_END - WS_CLR) / 16)) ((u4*)(ws + WS_CLR))[i] = (u4){~0u, ~0u, ~0u, ~0u}; }
    if (sj < 27) {
      float acc[8] = {0, 0, 0, 0, 0, 0, 0, 0};
      const int r0 = tid >> 3, c8 = tid & 7;
#pragma unroll
      for (int i = 0; i < 10; ++i) {
        const u4 v = *(const u4*)(win + (size_t)(r0 + 64 * i) * F_ + sj * 64 + c8 * 8);
#pragma unroll
        for (int k = 0; k < 4; ++k) { acc[2 * k] += bflo(v[k]); acc[2 * k + 1] += bfhi(v[k]); }
      }
      LAS float* red = (LAS float*)S;
#pragma unroll
      for (int k = 0; k < 8; ++k) red[r0 * 65 + c8 * 8 + k] = acc[k];
      __syncthreads();
      if (tid < 64) { float t = 0.f; for (int r = 0; r < 64; ++r) t += red[r * 65 + tid]; ((float*)(ws + WS_CS))[sj * 64 + tid] = t; }
    }
    return;
  }

  unsigned p_lar = 0, p_aim = 0, p_cr0 = 0, p_ci0 = 0, p_cr1 = 0, p_ci1 = 0, p_ldt = 0;
  const int kc = w >> 1, n = (w & 1) * 64 + lane;
  if (w < 6) {
    const int o = (d0 + kc) * NS;
    p_lar = lar[o + n]; p_aim = aim[o + n]; p_cr0 = cre[o + n]; p_ci0 = cim[o + n]; p_cr1 = cre[o + (n ^ 64)]; p_ci1 = cim[o + (n ^ 64)]; p_ldt = ldt[d0 + kc];
  }
  u2 xc[4];
  { const char* xb = (const char*)x + ((2 * d0) & ~3) + (size_t)(4 * tid) * (D_ * 2);
#pragma unroll
    for (int i = 0; i < 4; ++i) xc[i] = *(const u2*)(xb + i * (D_ * 2)); }
  const float dk0 = bf2f(dsk[d0]), dk1 = bf2f(dsk[d0 + 1]), dk2 = bf2f(dsk[d0 + 2]);

  STAMP(2);
  if (w < 6) {
    const float dt = rbf(expf(bf2f(p_ldt)));
    const float ar = -rbf(expf(bf2f(p_lar))) * dt, ai = bf2f(p_aim) * dt;
    float sa = fabsf(bf2f(p_cr0)) + fabsf(bf2f(p_ci0)) + fabsf(bf2f(p_cr1)) + fabsf(bf2f(p_ci1));
    sa = rowsum16(sa); sa += __shfl_xor(sa, 16); sa += __shfl_xor(sa, 32);
    sa *= dt;
    int e = 13 - (int)((__float_as_uint(sa) >> 23) & 0xff) + 127; e = max(-100, min(100, e));
    const float sc = __uint_as_float((unsigned)(e + 127) << 23);
    if (lane == 0 && !(w & 1)) *(LAS float*)(S + A_MISC + 4 + 4 * kc) = 2.f * __uint_as_float((unsigned)(127 - e) << 23);
    const f2 C = {bf2f(p_cr0) * dt * sc, bf2f(p_ci0) * dt * sc};
    const float ar2 = ar * 1.44269504088896340736f, rev = ai * 0.15915494309189533577f;
    const f2 E1 = cexpk(ar2, rev, 1.f), E8 = cexpk(ar2, rev, 8.f), E64 = cexpk(ar2, rev, 64.f), E512 = cexpk(ar2, rev, 512.f);
    const f2 E2 = csq(E1), E4 = csq(E2), E16 = csq(E8), E32 = csq(E16), E128 = csq(E64), E256 = csq(E128), E1024 = csq(E512);
    LAS char* tx = S + A_TAB + kc * A_TABCH + n * 4;
    LAS char* ty = tx + 32 * A_ROW;
#define PUTX(r, v) (*(LAS unsigned*)(tx + (r) * A_ROW) = pkh((v).x, -(v).y))
#define PUTY(r, v) (*(LAS unsigned*)(ty + (r) * A_ROW) = pkh((v).x, (v).y))
    { f2 Y[16];
      Y[0] = (f2){1.f, 0.f}; Y[1] = E8; Y[2] = E16; Y[3] = cmul(Y[1], E16); Y[4] = E32;
#pragma unroll
      for (int j = 1; j < 4; ++j) Y[4 + j] = cmul(Y[j], E32);
      Y[8] = E64;
#pragma unroll
      for (int j = 1; j < 8; ++j) Y[8 + j] = cmul(Y[j], E64);
#pragma unroll
      for (int j = 0; j < 16; ++j) PUTY(j, Y[j]);
      PUTY(16, E128);
#pragma unroll
      for (int j = 1; j < 16; ++j) { f2 t = cmul(Y[j], E128); PUTY(16 + j, t); } }
    { const f2 X1 = cmul(C, E1), X2 = cmul(C, E2), X3 = cmul(X1, E2), X4 = cmul(C, E4), X5 = cmul(X1, E4), X6 = cmul(X2, E4), X7 = cmul(X3, E4), X8 = cmul(C, E8);
      PUTX(0, C); PUTX(1, X1); PUTX(2, X2); PUTX(3, X3); PUTX(4, X4); PUTX(5, X5); PUTX(6, X6); PUTX(7, X7);
      const f2 X256 = cmul(C, E256), X258 = cmul(X2, E256), X260 = cmul(X4, E256), X262 = cmul(X6, E256), X264 = cmul(X8, E256);
      PUTX(8, X256); PUTX(9, X258); PUTX(10, X260); PUTX(11, X262); PUTX(16, X264);
      const f2 X512 = cmul(C, E512), X516 = cmul(X4, E512), X520 = cmul(X8, E512), X768 = cmul(X256, E512), X772 = cmul(X260, E512), X776 = cmul(X264, E512);
      PUTX(17, X512); PUTX(18, X516); PUTX(19, X520); PUTX(21, X768); PUTX(22, X772); PUTX(23, X776);
      f2 t;
      t = cmul(C, E1024); PUTX(24, t); t = cmul(X8, E1024); PUTX(25, t); t = cmul(X256, E1024); PUTX(26, t); t = cmul(X264, E1024); PUTX(27, t);
      t = cmul(X512, E1024); PUTX(28, t); t = cmul(X520, E1024); PUTX(29, t); t = cmul(X768, E1024); PUTX(30, t); t = cmul(X776, E1024); PUTX(31, t); }
  }
  STAMP(3);
  __syncthreads();
  STAMP(4);

  if (w < 3) {
    LAS char* ta = S + A_TAB + w * A_TABCH + (lane & 31) * A_ROW + (lane >> 5) * 16;
    f16v acc = {};
#pragma unroll
    for (int ks = 0; ks < 16; ++ks) {
      const h8 a = *(LAS h8*)(ta + ks * 32), b = *(LAS h8*)(ta + 32 * A_ROW + ks * 32);
      acc = __builtin_amdgcn_mfma_f32_32x32x16_f16(a, b, acc, 0, 0, 0);
    }
    LAS char* kb = S + w * A_TOEP + 32;
    const int j = lane & 31, hh = lane >> 5;
    *(LAS u2*)(kb + 2 * (8 * j + 4 * hh)) = (u2){pkh(acc[0], acc[1]), pkh(acc[2], acc[3])};
    if (hh == 0) *(LAS u4*)(kb + 2 * (256 + 8 * j)) = (u4){pkh(acc[4], acc[4]), pkh(acc[5], acc[6]), pkh(acc[6], acc[6]), pkh(acc[7], acc[8])};
    *(LAS u4*)(kb + 2 * (512 + 256 * hh + 8 * j)) = (u4){pkh(acc[9], acc[9]), pkh(acc[9], acc[10]), pkh(acc[10], acc[10]), pkh(acc[11], acc[11])};
    { const unsigned aa = pkh(acc[12], acc[12]), bb = pkh(acc[13], acc[13]), m = pkh((j & 1) ? acc[13] : acc[12], acc[13]);
      *(LAS u4*)(kb + 2 * (1024 + 512 * hh + 8 * j)) = (u4){aa, aa, m, bb}; }
    { const unsigned aa = pkh(acc[14], acc[14]), bb = pkh(acc[15], acc[15]), m = pkh((j & 1) ? acc[15] : acc[14], acc[15]);
      *(LAS u4*)(kb + 2 * (1280 + 512 * hh + 8 * j)) = (u4){aa, aa, m, bb}; }
    if (lane < 2) *(LAS u4*)(kb - 32 + 16 * lane) = (u4){0, 0, 0, 0};
    if (lane == 2) *(LAS u4*)(kb + 4096) = (u4){0, 0, 0, 0};
  }
  STAMP(5);
  unsigned st[4];
  { const unsigned tag = *(LAS unsigned*)(S + A_MISC);
    for (unsigned spins = 0;; ++spins) {
      bool ok = true;
#pragma unroll
      for (int i = 0; i < 4; ++i) { const unsigned long long g = __hip_atomic_load(g1 + 4 * tid + i, RLX_AGENT); st[i] = (unsigned)g; ok &= (unsigned)(g >> 32) == tag; }
      if (__all(ok) || spins > (1u << 20)) break;
      __builtin_amdgcn_s_sleep(1);
    } }
  STAMP(6);
  float uu[3][4], rr[3][4];
  { const bool sh = (d0 & 1);
#pragma unroll
    for (int i = 0; i < 4; ++i) {
      const float m = bflo(st[i]), rd = __builtin_amdgcn_rcpf(bfhi(st[i]));
      const float x0 = sh ? bfhi(xc[i].x) : bflo(xc[i].x), x1 = sh ? bflo(xc[i].y) : bfhi(xc[i].x), x2 = sh ? bfhi(xc[i].y) : bflo(xc[i].y);
      uu[0][i] = rbf(rbf(x0 - m) * rd); uu[1][i] = rbf(rbf(x1 - m) * rd); uu[2][i] = rbf(rbf(x2 - m) * rd);
      rr[0][i] = x0 + uu[0][i] * dk0; rr[1][i] = x1 + uu[1][i] * dk1; rr[2][i] = x2 + uu[2][i] * dk2;
    } }
  STAMP(7);
  __syncthreads();
  STAMP(8);
  if (bid == 0 && tid == 0) __hip_atomic_store((GAS unsigned*)(ws + WS_EPOCH), *(LAS unsigned*)(S + A_MISC), RLX_AGENT);
#pragma unroll
  for (int c = 0; c < 3; ++c) {
    *(LAS u2*)(S + A_UREV + c * 4608 + 2 * (2044 - 4 * tid)) = (u2){pkh(uu[c][3], uu[c][2]), pkh(uu[c][1], uu[c][0])};
    *(LAS f4*)(S + A_RT + c * 8192 + 16 * tid) = (f4){rr[c][0], rr[c][1], rr[c][2], rr[c][3]};
  }
  if (tid < 96) *(LAS u4*)(S + A_UREV + (tid >> 5) * 4608 + 4096 + 16 * (tid & 31)) = (u4){0, 0, 0, 0};
  for (int ck = tid; ck < 3 * 258; ck += 512) {
    const int c = ck / 258, i = ck - c * 258;
    LAS char* p = S + c * A_TOEP + 16 * i;
    const u4 a = *(LAS u4*)p; const u2 b = *(LAS u2*)(p + 16);
    const unsigned o0 = __builtin_amdgcn_alignbit(a.y, a.x, 16), o1 = __builtin_amdgcn_alignbit(a.z, a.y, 16), o2 = __builtin_amdgcn_alignbit(a.w, a.z, 16),
                   o3 = __builtin_amdgcn_alignbit(b.x, a.w, 16), o4 = __builtin_amdgcn_alignbit(b.y, b.x, 16);
    *(LAS u4*)(p + A_CS) = (u4){o0, o1, o2, o3};
    *(LAS u4*)(p + 2 * A_CS) = (u4){a.y, a.z, a.w, b.x};
    *(LAS u4*)(p + 3 * A_CS) = (u4){o1, o2, o3, o4};
  }
  STAMP(9);
  __syncthreads();
  STAMP(10);

  f4 acc[8];
  if (w < 4) {
#pragma unroll
    for (int q = 0; q < 8; ++q) acc[q] = (f4){0, 0, 0, 0};
    const int g = lane >> 4, qq = (lane >> 2) & 3, pp = lane & 3;
    const int aoff = ((1 + qq) & 3) * A_CS + 2 * (8 * g + ((1 + qq) & 4) + 4 * pp);
    const int boff = A_UREV + 4064 - 32 * (lane & 15) + 16 * g;
    h8 A0[8], B0[8], A1[8], B1[8];
    auto load = [&](int u, h8* A, h8* B) {
      const int ch = u >> 3, j0 = u & 7;
      LAS char* pa = S + ch * A_TOEP + aoff + 64 * j0;
      LAS char* pb = S + ch * 4608 + boff + 64 * j0;
#pragma unroll
      for (int m = 0; m < 8; ++m) {
        const s4 lo = __builtin_amdgcn_ds_read_tr16_b64_v4i16((LAS s4*)(pa + 512 * m)), hi = __builtin_amdgcn_ds_read_tr16_b64_v4i16((LAS s4*)(pa + 512 * m + 8));
        A[m] = __builtin_bit_cast(h8, __builtin_shufflevector(lo, hi, 0, 1, 2, 3, 4, 5, 6, 7));
        B[m] = *(LAS h8*)(pb - 512 * m);
      } };
    auto mma = [&](h8* A, h8* B) {
#pragma unroll
      for (int m = 0; m < 8; ++m)
#pragma unroll
        for (int q = m; q < 8; ++q) acc[q] = __builtin_amdgcn_mfma_f32_16x16x32_f16(A[m], B[q - m], acc[q], 0, 0, 0); };
    auto dump = [&](int e) {
#pragma unroll
      for (int q = 0; q < 8; ++q) { *(LAS f4*)(S + A_EX + e * 8192 + q * 1024 + lane * 16) = acc[q]; acc[q] = (f4){0, 0, 0, 0}; } };
    const int u0 = 6 * w;
    load(u0, A0, B0);
    load(u0 + 1, A1, B1); mma(A0, B0);
    load(u0 + 2, A0, B0); mma(A1, B1);
    if (w == 1) dump(0);
    load(u0 + 3, A1, B1); mma(A0, B0);
    load(u0 + 4, A0, B0); mma(A1, B1);
    if (w == 2) dump(1);
    load(u0 + 5, A1, B1); mma(A0, B0);
    mma(A1, B1);
    if (w == 2) dump(2);
  }
  STAMP(11);
  __syncthreads();
  STAMP(12);
  if (w < 4 && w != 2) {
    const int c = w == 3 ? 2 : w;
    if (c >= cfirst) {
      const float ys = *(LAS float*)(S + A_MISC + 4 + 4 * c);
      us* ht = (us*)(ws + WS_HT) + (size_t)(d0 + c) * L_;
      const int to = 16 * (lane & 15) + 4 * (lane >> 4);
#pragma unroll
      for (int q = 0; q < 8; ++q) {
        const f4 e = *(LAS f4*)(S + A_EX + c * 8192 + q * 1024 + lane * 16);
        const f4 r = *(LAS f4*)(S + A_RT + c * 8192 + 4 * (256 * q + to));
        const f4 h = (acc[q] + e) * ys + r;
        *(u2*)(ht + 256 * q + to) = (u2){pkbf(h.x, h.y), pkbf(h.z, h.w)};
      }
    }
  }
  STAMP(13); STAMP_OUT(ws, 0);
}

typedef u4 rsrc_t;
__device__ __forceinline__ rsrc_t mkrs(const void* p, unsigned bytes) {
  const unsigned long long a = (unsigned long long)p; return (u4){(unsigned)a, (unsigned)(a >> 32) & 0xffffu, bytes, 0x00020000u}; }
#define DMA16(rs, lp, vo, so) asm volatile("s_mov_b32 m0, %0\n\ts_nop 0\n\tbuffer_load_dwordx4 %1, %2, %3 offen lds" ::"s"((unsigned)(size_t)(lp)), "v"(vo), "s"(rs), "s"(so) : "m0")
#define VMBAR(n) asm volatile("s_waitcnt vmcnt(%0)\n\ts_barrier" ::"n"(n) : "memory")
#define BAR() asm volatile("s_barrier" ::: "memory")
__device__ __forceinline__ b8 trd(LAS char* lo, LAS char* hi) {
  const s4 a = __builtin_amdgcn_ds_read_tr16_b64_v4i16((LAS s4*)lo), b = __builtin_amdgcn_ds_read_tr16_b64_v4i16((LAS s4*)hi);
  return __builtin_bit_cast(b8, __builtin_shufflevector(a, b, 0, 1, 2, 3, 4, 5, 6, 7)); }
#define MFMA(a, b, c) __builtin_amdgcn_mfma_f32_16x16x32_bf16(a, b, c, 0, 0, 0)
__device__ __forceinline__ f2 gelu2(f2 x) {
  const f2 t = __builtin_elementwise_fma(x * x, (f2){-0.10294324f, -0.10294324f}, (f2){-2.30220819f, -2.30220819f});
  const f2 e = x * t;
  const f2 d = (f2){__builtin_amdgcn_exp2f(e.x), __builtin_amdgcn_exp2f(e.y)} + (f2){1.f, 1.f};
  return x * (f2){__builtin_amdgcn_rcpf(d.x), __builtin_amdgcn_rcpf(d.y)}; }
__device__ __forceinline__ f2 lnstat(float sum, float ss, float inv_n) {
  const float m = sum * inv_n; const float var = rbf(fmaxf(ss * inv_n - m * m, 0.f));
  return (f2){rbf(m), __builtin_amdgcn_rcpf(rbf(__builtin_sqrtf(rbf(var + 1e-5f))))}; }

constexpr int B_R = 9, B_SLOT = 16384, B_ST = B_R * B_SLOT, B_LDS = B_ST + 2048;
__global__ __launch_bounds__(256, 1) void kB(const us* __restrict__ win, const us* __restrict__ wout, char* __restrict__ ws) {
  extern __shared__ __attribute__((aligned(16))) char lds[];
  LAS char* S = (LAS char*)lds;
  const int tid = threadIdx.x, lane = tid & 63, w = __builtin_amdgcn_readfirstlane(tid >> 6), bid = blockIdx.x;
  const int xcd = bid & 7, slot = bid >> 3;
  const int c = lane & 15, g = lane >> 4, qq = (lane >> 2) & 3, pp = lane & 3;
  STAMP_DECL STAMP(0);
  if (slot >= 28) {
    const int sj = (slot - 28) * 8 + xcd;
    if (sj < 27) {
#pragma unroll
      for (int i = 0; i < 10; ++i) *(LAS u4*)(S + 16 * (tid + 256 * i)) = *(const u4*)((const char*)wout + sj * 40960 + 16 * (tid + 256 * i));
      __syncthreads();
#pragma unroll
      for (int i = 0; i < 10; ++i) { const int U = w + 4 * i; LAS char* p = S + (8 * g + qq) * 1280 + 32 * U + 8 * pp;
        *(b8*)(ws + WS_WO + (size_t)(sj * 40 + U) * 1024 + lane * 16) = trd(p, p + 4 * 1280); }
    }
    return;
  }
  const int P = 4 * (xcd >> 1) + slot / 7, G = 7 * (xcd & 1) + slot % 7;
  const bool spec = G == 13;
  const int u2b = spec ? 0 : 2 + 4 * G, u1b = spec ? 52 : 4 * G, nv = spec ? 2 : 4;
  const rsrc_t rA = mkrs(ws + WS_HT, D_ * L_ * 2), rB = mkrs(win, D_ * F_ * 2);
  const int r8 = (lane >> 2) & 7, sw = (2 * w + (r8 >> 2)) & 3, chl = 4 * (lane >> 5) + ((lane & 3) ^ sw);
  const int vA = (8 * w + r8) * 4096 + P * 256 + chl * 16;
  const int vB2 = (8 * w + r8) * 3456 + (FH + 16 * u2b) * 2 + (spec ? (chl & 3) : chl) * 16, vB1 = (8 * w + r8) * 3456 + 32 * u1b + chl * 16;
  auto issue = [&](int ks) {
    LAS char* d = S + (ks % B_R) * B_SLOT + 2048 * w;
    DMA16(rA, d, vA, ks * 131072); DMA16(rA, d + 1024, vA + 128, ks * 131072);
    DMA16(rB, d + 8192, vB2, ks * 110592); DMA16(rB, d + 9216, vB1, ks * 110592); };
  const float* cs = (const float*)(ws + WS_CS);
  float cs2[4]; f4 cs1[4];
#pragma unroll
  for (int u = 0; u < 4; ++u) { cs2[u] = cs[FH + 16 * (u2b + u) + c]; cs1[u] = *(const f4*)(cs + 16 * (u1b + u) + 4 * g); }
#pragma unroll
  for (int ks = 0; ks < B_R - 1; ++ks) issue(ks);
  int rv[4];
#pragma unroll
  for (int v = 0; v < 4; ++v) { const int par = v >> 1, hi = v & 1; rv[v] = 2048 * g + 64 * (4 * hi + qq) + 16 * ((2 * par + (pp >> 1)) ^ ((2 * g + hi) & 3)) + 8 * (pp & 1); }
  b8 fa[2][2], fb[2][8];
  auto rd = [&](int ks) {
    LAS char* s = S + (ks % B_R) * B_SLOT; const int b = ks & 1;
#pragma unroll
    for (int q = 0; q < 2; ++q) fa[b][q] = trd(s + 512 * w + rv[2 * q], s + 512 * w + rv[2 * q + 1]);
#pragma unroll
    for (int u = 0; u < 8; ++u) fb[b][u] = trd(s + 8192 + 512 * (u >> 1) + rv[2 * (u & 1)], s + 8192 + 512 * (u >> 1) + rv[2 * (u & 1) + 1]); };
  f4 a2[4][2], a1[4][2], aS[2], aM[2];
#pragma unroll
  for (int q = 0; q < 2; ++q) { aS[q] = aM[q] = (f4){0, 0, 0, 0};
#pragma unroll
    for (int u = 0; u < 4; ++u) a2[u][q] = a1[u][q] = (f4){0, 0, 0, 0}; }
  const b8 ones = __builtin_bit_cast(b8, (u4){0x3f803f80u, 0x3f803f80u, 0x3f803f80u, 0x3f803f80u});
  STAMP(1);
  VMBAR(4 * (B_R - 2));
  STAMP(2);
  rd(0);
#pragma unroll
  for (int ks = 0; ks < 20; ++ks) {
    __builtin_amdgcn_sched_barrier(0);
    if (ks + 1 < 20) {
      { const int last = ks + B_R - 2 < 19 ? ks + B_R - 2 : 19; VMBAR(4 * (last - (ks + 1))); }
      if (ks + B_R - 1 < 20) issue(ks + B_R - 1);
      rd(ks + 1);
    }
    const int b = ks & 1;
#pragma unroll
    for (int u = 0; u < 4; ++u)
#pragma unroll
      for (int q = 0; q < 2; ++q) { a2[u][q] = MFMA(fa[b][q], fb[b][u], a2[u][q]); a1[u][q] = MFMA(fb[b][4 + u], fa[b][q], a1[u][q]); }
#pragma unroll
    for (int q = 0; q < 2; ++q) { aS[q] = MFMA(fa[b][q], fa[b][q], aS[q]); aM[q] = MFMA(fa[b][q], ones, aM[q]); }
    if (ks + 1 < 20) {
#pragma unroll
      for (int i = 0; i < 20; ++i) { __builtin_amdgcn_sched_group_barrier(0x008, 1, 0); __builtin_amdgcn_sched_group_barrier(0x100, 1, 0); }
    }
  }
  __builtin_amdgcn_sched_barrier(0);
  STAMP(3);
  LAS char* st = S + B_ST + 256 * w;
#pragma unroll
  for (int q = 0; q < 2; ++q) {
    const int r = c & 3;
    const float ss = r == 0 ? aS[q][0] : r == 1 ? aS[q][1] : r == 2 ? aS[q][2] : aS[q][3];
    const float sm = r == 0 ? aM[q][0] : r == 1 ? aM[q][1] : r == 2 ? aM[q][2] : aM[q][3];
    const f2 v = lnstat(sm, ss, 1.f / D_);
    if ((c >> 2) == g) *(LAS f2*)(st + 8 * (16 * q + c)) = v;
  }
  unsigned zp[4][2][2];
  GAS unsigned long long* zs = (GAS unsigned long long*)(ws + WS_ZS) + (size_t)P * 16 * 128 + 32 * w;
#pragma unroll
  for (int q = 0; q < 2; ++q) {
    const f4 s01 = *(LAS f4*)(st + 8 * (16 * q + 4 * g)), s23 = *(LAS f4*)(st + 8 * (16 * q + 4 * g) + 16);
    const f2 m01 = {-s01.x, -s01.z}, r01 = {s01.y, s01.w}, m23 = {-s23.x, -s23.z}, r23 = {s23.y, s23.w};
    f2 sm01 = {0, 0}, sm23 = {0, 0}, sq01 = {0, 0}, sq23 = {0, 0};
#pragma unroll
    for (int u = 0; u < 4; ++u) {
      const f2 cc = {cs2[u], cs2[u]};
      const f2 x01 = gelu2(__builtin_elementwise_fma(m01, cc, a2[u][q].xy) * r01), x23 = gelu2(__builtin_elementwise_fma(m23, cc, a2[u][q].zw) * r23);
      zp[u][q][0] = pkbf(x01.x, x01.y); zp[u][q][1] = pkbf(x23.x, x23.y);
      if (u < 2 || !spec) {
        const f2 y01 = {bflo(zp[u][q][0]), bfhi(zp[u][q][0])}, y23 = {bflo(zp[u][q][1]), bfhi(zp[u][q][1])};
        sm01 += y01; sm23 += y23; sq01 = __builtin_elementwise_fma(y01, y01, sq01); sq23 = __builtin_elementwise_fma(y23, y23, sq23); }
    }
    const float t0 = rowsum16(sm01.x), t1 = rowsum16(sm01.y), t2 = rowsum16(sm23.x), t3 = rowsum16(sm23.y);
    const float v0 = rowsum16(sq01.x), v1 = rowsum16(sq01.y), v2 = rowsum16(sq23.x), v3 = rowsum16(sq23.y);
    const float ts = c == 0 ? t0 : c == 1 ? t1 : c == 2 ? t2 : t3, vs = c == 0 ? v0 : c == 1 ? v1 : c == 2 ? v2 : v3;
    if (c < 4) __hip_atomic_store(zs + G * 128 + 16 * q + 4 * g + c, ((unsigned long long)(__float_as_uint(vs) & 0x7fffffffu) << 32) | __float_as_uint(ts), RLX_AGENT);
  }
  STAMP(4);
  { const f2 s0 = *(LAS f2*)(st + 8 * c), s1 = *(LAS f2*)(st + 8 * (16 + c));
#pragma unroll
    for (int u = 0; u < 4; ++u) {
      const f2 a = gelu2(__builtin_elementwise_fma(-s0.xx, cs1[u].xy, a1[u][0].xy) * s0.yy), b = gelu2(__builtin_elementwise_fma(-s0.xx, cs1[u].zw, a1[u][0].zw) * s0.yy);
      const f2 d = gelu2(__builtin_elementwise_fma(-s1.xx, cs1[u].xy, a1[u][1].xy) * s1.yy), e = gelu2(__builtin_elementwise_fma(-s1.xx, cs1[u].zw, a1[u][1].zw) * s1.yy);
      if (u < nv) *(u4*)(ws + WS_Z1 + (size_t)((4 * P + w) * 54 + u1b + u) * 1024 + lane * 16) = (u4){pkbf(a.x, a.y), pkbf(b.x, b.y), pkbf(d.x, d.y), pkbf(e.x, e.y)};
    } }
  STAMP(5);
  { unsigned long long gv[7];
    GAS unsigned long long* src = zs + (lane >> 5) * 7 * 128 + (lane & 31);
    for (unsigned spins = 0;; ++spins) {
      bool ok = true;
#pragma unroll
      for (int i = 0; i < 7; ++i) { gv[i] = __hip_atomic_load(src + i * 128, RLX_AGENT); ok &= (unsigned)(gv[i] >> 32) != 0xffffffffu; }
      if (__all(ok) || spins > (1u << 20)) break;
      __builtin_amdgcn_s_sleep(1);
    }
    float sm = 0.f, sq = 0.f;
#pragma unroll
    for (int i = 0; i < 7; ++i) { sm += __uint_as_float((unsigned)gv[i]); sq += __uint_as_float((unsigned)(gv[i] >> 32)); }
    const float sm2 = __shfl_xor(sm, 32), sq2 = __shfl_xor(sq, 32);
    const f2 v = lane < 32 ? lnstat(sm + sm2, sq + sq2, 1.f / FH) : lnstat(sm2 + sm, sq2 + sq, 1.f / FH);
    if (lane < 32) *(LAS f2*)(st + 1024 + 8 * lane) = v; }
  STAMP(6);
#pragma unroll
  for (int q = 0; q < 2; ++q) {
    const f4 s01 = *(LAS f4*)(st + 1024 + 8 * (16 * q + 4 * g)), s23 = *(LAS f4*)(st + 1024 + 8 * (16 * q + 4 * g) + 16);
    const f2 m01 = {s01.x, s01.z}, r01 = {s01.y, s01.w}, m23 = {s23.x, s23.z}, r23 = {s23.y, s23.w};
#pragma unroll
    for (int u = 0; u < 4; ++u) {
      const f2 a = ((f2){bflo(zp[u][q][0]), bfhi(zp[u][q][0])} - m01) * r01, b = ((f2){bflo(zp[u][q][1]), bfhi(zp[u][q][1])} - m23) * r23;
      if (u < nv) *(u2*)(ws + WS_Z2 + (size_t)((4 * P + w) * 54 + u2b + u) * 1024 + ((2 * q + (g >> 1)) * 16 + c) * 16 + 8 * (g & 1)) = (u2){pkbf(a.x, a.y), pkbf(b.x, b.y)};
    }
  }
  STAMP(7); STAMP_OUT(ws, 1);
}

constexpr int C_R = 5, C_SLOT = 30720, C_LDS = C_R * C_SLOT;
__global__ __launch_bounds__(256, 1) void kC(const us* __restrict__ wsp, const us* __restrict__ bsp, char* __restrict__ ws) {
  extern __shared__ __attribute__((aligned(16))) char lds[];
  LAS char* S = (LAS char*)lds;
  const int tid = threadIdx.x, lane = tid & 63, w = __builtin_amdgcn_readfirstlane(tid >> 6), bid = blockIdx.x;
  const int xcd = bid & 7, slot = bid >> 3;
  const int hf = xcd & 1, P = 4 * (xcd >> 1) + (slot >> 3), Cg = slot & 7;
  const int c = lane & 15, g = lane >> 4;
  STAMP_DECL STAMP(0);
  const rsrc_t rW = mkrs(wsp, L_ * L_ * 2), rZ = mkrs(ws + WS_Z2, 64 * 54 * 1024);
  const int r8 = lane >> 3;
  const int vW0 = (128 * P + 32 * w + r8) * 4096 + hf * 2048 + ((lane & 7) ^ (r8 >> 1)) * 16, vW1 = (128 * P + 32 * w + 8 + r8) * 4096 + hf * 2048 + ((lane & 7) ^ (4 + (r8 >> 1))) * 16;
  const int vZ = lane * 16;
  int zo[4];
#pragma unroll
  for (int j = 0; j < 4; ++j) { const int n = w + 4 * j; zo[j] = ((32 * hf + n / 7) * 54 + 7 * Cg + n % 7) * 1024; }
  auto issue = [&](int s) {
    LAS char* d = S + (s % C_R) * C_SLOT;
    DMA16(rW, d + 4096 * w, vW0, s * 128); DMA16(rZ, d + 16384 + 1024 * w, vZ, zo[0] + s * 110592);
    DMA16(rW, d + 4096 * w + 1024, vW1, s * 128); DMA16(rZ, d + 16384 + 1024 * (w + 4), vZ, zo[1] + s * 110592);
    DMA16(rW, d + 4096 * w + 2048, vW0, s * 128 + 65536); DMA16(rZ, d + 16384 + 1024 * (w + 8), vZ, zo[2] + s * 110592);
    DMA16(rW, d + 4096 * w + 3072, vW1, s * 128 + 65536);
    if (w < 2) DMA16(rZ, d + 16384 + 1024 * (w + 12), vZ, zo[3] + s * 110592); };
  u4 z1[7]; float bs[2];
#pragma unroll
  for (int u = 0; u < 7; ++u) z1[u] = *(const u4*)(ws + WS_Z1 + (size_t)((4 * P + w) * 54 + min(7 * Cg + u, 53)) * 1024 + lane * 16);
#pragma unroll
  for (int q = 0; q < 2; ++q) bs[q] = hf ? bf2f(bsp[128 * P + 32 * w + 16 * q + c]) : 0.f;
#pragma unroll
  for (int s = 0; s < C_R - 1; ++s) issue(s);
  const int ra0 = (32 * w + c) * 128 + 16 * (g ^ (c >> 1)), ra1 = (32 * w + c) * 128 + 16 * ((4 + g) ^ (c >> 1));
  b8 fw[2][2], fz[2][7];
  auto rd = [&](int ks) {
    LAS char* s = S + ((ks >> 1) % C_R) * C_SLOT; const int b = ks & 1;
#pragma unroll
    for (int q = 0; q < 2; ++q) fw[b][q] = *(LAS b8*)(s + (b ? ra1 : ra0) + 2048 * q);
#pragma unroll
    for (int u = 0; u < 7; ++u) fz[b][u] = *(LAS b8*)(s + 16384 + (7 * b + u) * 1024 + lane * 16); };
  f4 acc[7][2];
#pragma unroll
  for (int u = 0; u < 7; ++u) acc[u][0] = acc[u][1] = (f4){0, 0, 0, 0};
#define CWAIT(n) do { if (w < 2) VMBAR(8 * (n)); else VMBAR(7 * (n)); } while (0)
  STAMP(1);
  CWAIT(C_R - 2);
  STAMP(2);
  rd(0);
#pragma unroll
  for (int ks = 0; ks < 32; ++ks) {
    __builtin_amdgcn_sched_barrier(0);
    if (ks + 1 < 32) {
      if (ks & 1) {
        const int s = ks >> 1;
        { const int last = s + C_R - 2 < 15 ? s + C_R - 2 : 15; CWAIT(last - (s + 1)); }
        if (s + C_R - 1 < 16) issue(s + C_R - 1);
        __builtin_amdgcn_sched_barrier(0);
      }
      rd(ks + 1);
    }
    const int b = ks & 1;
#pragma unroll
    for (int u = 0; u < 7; ++u)
#pragma unroll
      for (int q = 0; q < 2; ++q) acc[u][q] = MFMA(fz[b][u], fw[b][q], acc[u][q]);
    if (ks + 1 < 32) {
#pragma unroll
      for (int i = 0; i < 9; ++i) { __builtin_amdgcn_sched_group_barrier(0x008, 1, 0); __builtin_amdgcn_sched_group_barrier(0x100, 1, 0); }
    }
  }
  __builtin_amdgcn_sched_barrier(0);
  STAMP(3);
#pragma unroll
  for (int u = 0; u < 7; ++u) {
    const int U = 7 * Cg + u;
    if (U < 54) {
#pragma unroll
      for (int q = 0; q < 2; ++q) {
        const f4 gg = acc[u][q] + bs[q];
        const unsigned za = z1[u][2 * q], zb = z1[u][2 * q + 1];
        *(u2*)(ws + WS_P + (size_t)((hf * 128 + 8 * P + 2 * w + q) * 27 + (U >> 1)) * 1024 + ((2 * (U & 1) + (g >> 1)) * 16 + c) * 16 + 8 * (g & 1)) =
            (u2){pkbf(bflo(za) * gg.x, bfhi(za) * gg.y), pkbf(bflo(zb) * gg.z, bfhi(zb) * gg.w)};
      }
    }
  }
  STAMP(4); STAMP_OUT(ws, 2);
}

constexpr int D_H = 135 * 1024, D_LDS = D_H + 12 * 1024, D_PF = 14;
__global__ __launch_bounds__(512, 2) void kD(char* __restrict__ ws, us* __restrict__ out) {
  extern __shared__ __attribute__((aligned(16))) char lds[];
  LAS char* S = (LAS char*)lds;
  const int tid = threadIdx.x, lane = tid & 63, w = __builtin_amdgcn_readfirstlane(tid >> 6), bid = blockIdx.x;
  const int xcd = bid & 7, slot = bid >> 3;
  const int tp = 4 * xcd + (slot >> 3), dg = slot & 7;
  STAMP_DECL STAMP(0);
  if (w >= 4) {
    const int lw = w - 4;
    const rsrc_t rH = mkrs(ws + WS_HT, D_ * L_ * 2), rO = mkrs(ws + WS_WO, 27 * 40 * 1024);
#pragma unroll
    for (int i = 0; i < 3; ++i) { const int n = lw + 4 * i; DMA16(rH, S + D_H + 1024 * n, (lane >> 3) * 4096 + (lane & 7) * 16, (80 * dg + 8 * min(n, 9)) * 4096 + tp * 128); }
#pragma unroll
    for (int i = 0; i < 34; ++i) { const int n = min(lw + 4 * i, 134); DMA16(rO, S + 1024 * n, lane * 16, ((n / 5) * 40 + 5 * dg + n % 5) * 1024); }
#pragma unroll
    for (int ph = 0; ph < 7; ++ph) VMBAR(ph < 6 ? 34 - 5 * (ph + 1) : 0);
    STAMP(1); STAMP_OUT(ws, 3);
    return;
  }
  const int c = lane & 15, g = lane >> 4;
  const char* pa = ws + WS_P + (size_t)(4 * tp + w) * 27 * 1024 + lane * 16;
  const char* pb = pa + (size_t)128 * 27 * 1024;
  b8 fa[27], fb[27];
#pragma unroll
  for (int ks = 0; ks < D_PF; ++ks) { fa[ks] = *(const b8*)(pa + ks * 1024); fb[ks] = *(const b8*)(pb + ks * 1024); }
  f4 acc[5];
#pragma unroll
  for (int u = 0; u < 5; ++u) acc[u] = (f4){0, 0, 0, 0};
  STAMP(1);
#pragma unroll
  for (int ks = 0; ks < 27; ++ks) {
    __builtin_amdgcn_sched_barrier(0);
    if ((ks & 3) == 0) BAR();
    if (ks + D_PF < 27) { fa[ks + D_PF] = *(const b8*)(pa + (ks + D_PF) * 1024); fb[ks + D_PF] = *(const b8*)(pb + (ks + D_PF) * 1024); }
    b8 fw[5];
#pragma unroll
    for (int u = 0; u < 5; ++u) fw[u] = *(LAS b8*)(S + (5 * ks + u) * 1024 + lane * 16);
#pragma unroll
    for (int u = 0; u < 5; ++u) acc[u] = MFMA(fw[u], fa[ks], acc[u]);
#pragma unroll
    for (int u = 0; u < 5; ++u) acc[u] = MFMA(fw[u], fb[ks], acc[u]);
  }
  __builtin_amdgcn_sched_barrier(0);
  STAMP(2);
  us* o = out + (size_t)(64 * tp + 16 * w + c) * D_ + 80 * dg + 4 * g;
#pragma unroll
  for (int u = 0; u < 5; ++u) {
    const u2 h = __builtin_bit_cast(u2, __builtin_amdgcn_ds_read_tr16_b64_v4i16((LAS s4*)(S + D_H + (16 * u + 4 * g + ((lane >> 2) & 3)) * 128 + 32 * w + 8 * (lane & 3))));
    *(u2*)(o + 16 * u) = (u2){pkbf(bflo(h.x) + acc[u].x, bfhi(h.x) + acc[u].y), pkbf(bflo(h.y) + acc[u].z, bfhi(h.y) + acc[u].w)};
  }
  STAMP(3); STAMP_OUT(ws, 3);
}

extern "C" void kernel_launch(void* const* d_in, const int* in_sizes, int n_in, void* d_out, int out_size, void* d_ws, size_t ws_size, hipStream_t stream) {
  const us* x = (const us*)d_in[0];
  char* ws = (char*)d_ws;
  (void)hipFuncSetAttribute((const void*)kA, hipFuncAttributeMaxDynamicSharedMemorySize, A_LDS);
  (void)hipFuncSetAttribute((const void*)kB, hipFuncAttributeMaxDynamicSharedMemorySize, B_LDS);
  (void)hipFuncSetAttribute((const void*)kC, hipFuncAttributeMaxDynamicSharedMemorySize, C_LDS);
  (void)hipFuncSetAttribute((const void*)kD, hipFuncAttributeMaxDynamicSharedMemorySize, D_LDS);
  kA<<<256, 512, A_LDS, stream>>>(x, (const us*)d_in[1], (const us*)d_in[2], (const us*)d_in[3], (const us*)d_in[4], (const us*)d_in[5], (const us*)d_in[6], (const us*)d_in[8], ws);
  kB<<<256, 256, B_LDS, stream>>>((const us*)d_in[8], (const us*)d_in[11], ws);
  kC<<<256, 256, C_LDS, stream>>>((const us*)d_in[9], (const us*)d_in[10], ws);
  kD<<<256, 512, D_LDS, stream>>>(ws, (us*)d_out);
}
```

```cpp
#define SPLIT 3
#define D_PACE 5
#define TLBPF 65536
#define PW 1
#define WT 6
#define WSM 0
#include <hip/hip_runtime.h>
#include <hip/hip_bf16.h>
#include <stdint.h>

typedef float f2 __attribute__((ext_vector_type(2)));
typedef float f4 __attribute__((ext_vector_type(4)));
typedef float f16v __attribute__((ext_vector_type(16)));
typedef _Float16 h2 __attribute__((ext_vector_type(2)));
typedef _Float16 h4 __attribute__((ext_vector_type(4)));
typedef _Float16 h8 __attribute__((ext_vector_type(8)));
typedef __bf16 b2 __attribute__((ext_vector_type(2)));
typedef __bf16 b8 __attribute__((ext_vector_type(8)));
typedef short s4 __attribute__((ext_vector_type(4)));
typedef unsigned u2 __attribute__((ext_vector_type(2)));
typedef unsigned u4 __attribute__((ext_vector_type(4)));
typedef unsigned short us;
#define LAS __attribute__((address_space(3)))
#define GAS __attribute__((address_space(1)))
#define RLX_AGENT __ATOMIC_RELAXED, __HIP_MEMORY_SCOPE_AGENT

#define STAMP_DECL
#define STAMP(i)
#define STAMP_OUT(ws, kid)

#define A_GQ 12
#define A_G0 5
#define A_PS 12
#define F_SLP 3
constexpr int L_ = 2048, D_ = 640, NS = 128, F_ = 1728, FH = 864;

constexpr size_t WS_EPOCH = 0;
constexpr size_t WS_FA    = 576 << 10;
constexpr size_t WS_FB    = WS_FA + 32768;
constexpr size_t WS_FC    = WS_FB + 32768;
constexpr size_t WS_CS    = 4096;
constexpr size_t WS_ZS    = 65536;
constexpr size_t WS_HT    = 1 << 20;
constexpr size_t WS_Z1    = 4 << 20;
constexpr size_t WS_Z2    = 8 << 20;
constexpr size_t WS_P     = 12 << 20;
constexpr size_t WS_WO    = 20 << 20;
constexpr int F_DUM = 163584, F_LDS = 163840;

__device__ __forceinline__ float bf2f(unsigned v16) { return __uint_as_float(v16 << 16); }
__device__ __forceinline__ float bflo(unsigned v) { return __uint_as_float(v << 16); }
__device__ __forceinline__ float bfhi(unsigned v) { return __uint_as_float(v & 0xffff0000u); }
__device__ __forceinline__ unsigned pkbf(float a, float b) { f2 v = {a, b}; b2 r = __builtin_convertvector(v, b2); return __builtin_bit_cast(unsigned, r); }
__device__ __forceinline__ unsigned pkh(float a, float b) { f2 v = {a, b}; h2 r = __builtin_convertvector(v, h2); return __builtin_bit_cast(unsigned, r); }
__device__ __forceinline__ float rbf(float a) { return bflo(pkbf(a, a)); }
__device__ __forceinline__ f2 cmul(f2 x, f2 y) { f2 t = x * y.xx; f2 ys = {-y.y, y.y}; return __builtin_elementwise_fma(x.yx, ys, t); }
__device__ __forceinline__ f2 csq(f2 x) { f2 r; r.x = x.x * x.x - x.y * x.y; r.y = 2.f * x.x * x.y; return r; }
__device__ __forceinline__ f2 cexpk(float ar2, float rev, float p) {
  float m = __builtin_amdgcn_exp2f(ar2 * p); float f = __builtin_amdgcn_fractf(rev * p);
  f2 r = {m * __builtin_amdgcn_cosf(f), m * __builtin_amdgcn_sinf(f)}; return r; }
template <int CTRL> __device__ __forceinline__ float dppf(float v) { return __builtin_bit_cast(float, __builtin_amdgcn_update_dpp(0, __builtin_bit_cast(int, v), CTRL, 0xf, 0xf, true)); }
__device__ __forceinline__ float rowsum16(float v) {
  v += dppf<0x128>(v); v += dppf<0x124>(v); v += dppf<0x122>(v); v += dppf<0x121>(v); return v; }

typedef u4 rsrc_t;
typedef __amdgpu_buffer_rsrc_t rsrc_t_b;
__device__ __forceinline__ rsrc_t mkrs(const void* p, unsigned bytes) {
  const unsigned long long a = (unsigned long long)p; return (u4){(unsigned)a, (unsigned)(a >> 32) & 0xffffu, bytes, 0x00020000u}; }
#define DMA16(rs, lp, vo, so) asm volatile("s_mov_b32 m0, %0\n\ts_nop 0\n\tbuffer_load_dwordx4 %1, %2, %3 offen lds" ::"s"((unsigned)(size_t)(lp)), "v"(vo), "s"(rs), "s"(so) : "m0")
#define DMA16S(rs, lp, vo, so) asm volatile("s_mov_b32 m0, %0\n\ts_nop 0\n\tbuffer_load_dwordx4 %1, %2, %3 offen sc1 lds" ::"s"((unsigned)(size_t)(lp)), "v"(vo), "s"(rs), "s"(so) : "m0")
#define STAUX 16
#define VMBAR(n) asm volatile("s_waitcnt vmcnt(%0)\n\ts_barrier" ::"n"(n) : "memory")
#define LP(off) ((LAS char*)(unsigned)(off))
#define BAR() asm volatile("s_barrier" ::: "memory")
#define LGBAR() asm volatile("s_waitcnt lgkmcnt(0)\n\ts_barrier" ::: "memory")
__device__ __forceinline__ float sel4(int r, float a, float b, float c, float d) {
  asm volatile("" : "+v"(a), "+v"(b), "+v"(c), "+v"(d)); const float lo = (r & 1) ? b : a, hi = (r & 1) ? d : c; return (r & 2) ? hi : lo; }
__device__ __forceinline__ b8 trd(LAS char* lo, LAS char* hi) {
  const s4 a = __builtin_amdgcn_ds_read_tr16_b64_v4i16((LAS s4*)lo), b = __builtin_amdgcn_ds_read_tr16_b64_v4i16((LAS s4*)hi);
  return __builtin_bit_cast(b8, __builtin_shufflevector(a, b, 0, 1, 2, 3, 4, 5, 6, 7)); }
#define MFMA(a, b, c) __builtin_amdgcn_mfma_f32_16x16x32_bf16(a, b, c, 0, 0, 0)
__device__ __forceinline__ f2 gelu2(f2 x) {
  const f2 t = __builtin_elementwise_fma(x * x, (f2){-0.10294324f, -0.10294324f}, (f2){-2.30220819f, -2.30220819f});
  const f2 e = x * t;
  const f2 d = (f2){__builtin_amdgcn_exp2f(e.x), __builtin_amdgcn_exp2f(e.y)} + (f2){1.f, 1.f};
  return x * (f2){__builtin_amdgcn_rcpf(d.x), __builtin_amdgcn_rcpf(d.y)}; }
__device__ __forceinline__ f2 lnstat(float sum, float ss, float inv_n) {
  const float m = sum * inv_n; const float var = rbf(fmaxf(ss * inv_n - m * m, 0.f));
  return (f2){rbf(m), __builtin_amdgcn_rcpf(rbf(__builtin_amdgcn_sqrtf(rbf(var + 1e-5f))))}; }

#define CODE_ADDR(lo, hi, sym) asm volatile("s_getpc_b64 s[20:21]\n\ts_add_u32 s20, s20, " sym "@rel32@lo+4\n\ts_addc_u32 s21, s21, " sym "@rel32@hi+12\n\ts_mov_b32 %0, s20\n\ts_mov_b32 %1, s21" : "=s"(lo), "=s"(hi)::"s20", "s21", "scc")
#define DMA4(rs, lp, vo, so) asm volatile("s_mov_b32 m0, %0\n\ts_nop 0\n\tbuffer_load_dword %1, %2, %3 offen lds" ::"s"((unsigned)(size_t)(lp)), "v"(vo), "s"(rs), "s"(so) : "m0")

#define MKB(p, n) __builtin_amdgcn_make_buffer_rsrc((void*)(p), 0, n, 0x00020000)
__device__ __forceinline__ void st16(rsrc_t_b r, int vo, int so, u4 v) { __builtin_amdgcn_raw_buffer_store_b128(v, r, vo + so, 0, STAUX); }
__device__ __forceinline__ u4 pair16(u2 a, u2 b) {
  const auto x = __builtin_amdgcn_permlane16_swap(a.x, b.x, false, false), y = __builtin_amdgcn_permlane16_swap(a.y, b.y, false, false);
  return (u4){x[0], y[0], x[1], y[1]}; }
__device__ __forceinline__ void publish(char* f, int idx, unsigned tag) { __hip_atomic_store((GAS unsigned*)f + 32 * idx, tag, RLX_AGENT); }
__device__ __forceinline__ void wait_flags(char* f, int n, int lane, unsigned tag) {
  const rsrc_t_b r = MKB(f, 8192);
  bool ok = lane >= n;
  for (unsigned spins = 0;; ++spins) {
    int vo = lane * 128; asm volatile("" : "+v"(vo));
    if (!ok) ok = __builtin_amdgcn_raw_buffer_load_b32(r, vo, 0, 16) == tag;
    if (__all(ok) || spins > (1u << 18)) break;
    asm volatile("s_sleep %0" ::"n"(F_SLP) : "memory");
  }
  asm volatile("" ::: "memory"); }
constexpr int A_ROW = 528;
constexpr int A_TABCH = 59 * A_ROW;
constexpr int A_T0 = 3 * A_TABCH;
constexpr int A_CH = 4224;
constexpr int A_UREV = A_T0 + 3 * A_CH;
constexpr int A_UC = 4512, A_UCH = 4 * A_UC;
constexpr int A_MISC = A_UREV + 3 * A_UCH;
constexpr int A_ST = A_MISC + 64;
constexpr int A_DUM = F_DUM;
constexpr int A_EX = 0;
constexpr size_t WS_X2 = 24 << 20;
constexpr unsigned F_CODE = 36352;
constexpr unsigned A_CODE = 13824;
typedef unsigned u3 __attribute__((ext_vector_type(3)));
#define A_NE 2
#define LDSW(off) (*(volatile LAS unsigned*)LP(off))
__device__ __forceinline__ float swap32add(float a, float b) {
  const auto r = __builtin_amdgcn_permlane32_swap(__float_as_uint(a), __float_as_uint(b), false, false); return __uint_as_float(r[0]) + __uint_as_float(r[1]); }
__device__ __forceinline__ float swap16add(float a) {
  const auto r = __builtin_amdgcn_permlane16_swap(__float_as_uint(a), __float_as_uint(a), false, false); return __uint_as_float(r[0]) + __uint_as_float(r[1]); }
__device__ __forceinline__ unsigned lnh(unsigned d, float mr, float rd) {
  const f2 v = __builtin_elementwise_fma((f2){bflo(d), bfhi(d)}, (f2){rd, rd}, (f2){mr, mr}); return pkh(v.x, v.y); }
__device__ __forceinline__ float hlo(unsigned v) { return (float)__builtin_bit_cast(h2, v).x; }
__device__ __forceinline__ float hhi(unsigned v) { return (float)__builtin_bit_cast(h2, v).y; }

template <int P0, int P1> __device__ __forceinline__ void body(const us* __restrict__ x, const us* __restrict__ lar, const us* __restrict__ aim,
    const us* __restrict__ cre, const us* __restrict__ cim, const us* __restrict__ ldt, const us* __restrict__ dsk, const us* __restrict__ win,
    const us* __restrict__ wsp, const us* __restrict__ bsp, const us* __restrict__ wout, us* __restrict__ out, char* __restrict__ ws) {
  extern __shared__ __attribute__((aligned(16))) char lds[];
  LAS char* S = (LAS char*)lds;
  const int tid = threadIdx.x, lane = tid & 63, w = __builtin_amdgcn_readfirstlane(tid >> 6), bid = blockIdx.x;
  const int slot = bid >> 3, cb = (bid & 7) * 27 + slot;
  const bool conv = slot < 27 && cb < 214;
  const int d0 = 3 * cb;
  unsigned tag;
  const int xcd = bid & 7;
  auto touch_wsp = [&]() { const int ln = slot * 256 + (w & 3) * 64 + lane;
      DMA4(mkrs(wsp, L_ * L_ * 2), S + F_DUM, (512 * (xcd >> 1) + (ln >> 4)) * 4096 + (xcd & 1) * 2048 + (ln & 15) * 128, 0); };
  if constexpr (P0 > 0) tag = 0;
  else {
  STAMP_DECL STAMP(0);
  __builtin_amdgcn_s_setprio(3);
  const int ii = lane & 7, ps = lane >> 3, p1 = 64 + 8 * w + ps;
  const rsrc_t_b rx = __builtin_amdgcn_make_buffer_rsrc((void*)x, 0, L_ * D_ * 2, 0x00020000);
  u3 xr[2];
  { const int ro = (256 * (bid >> 5) + 32 * (bid & 7) + ((bid >> 3) & 3) + 4 * ii) * 1280, vo = ro + 96 * w + 12 * ps;
    xr[0] = __builtin_amdgcn_raw_buffer_load_b96(rx, vo, 0, 0);
    xr[1] = __builtin_amdgcn_raw_buffer_load_b96(rx, p1 < 106 ? vo + 768 : p1 == 106 ? ro + 1268 : 0x7f000000, 0, 0); }
  if (w == 7) { unsigned clo, chi; CODE_ADDR(clo, chi, "kF"); const rsrc_t rc = {clo, chi & 0xffffu, A_CODE, 0x00020000u};
    DMA4(rc, S + A_DUM, lane * 128, 0); DMA4(rc, S + A_DUM, lane * 128, 8192); }
  tag = __hip_atomic_load((GAS unsigned*)(ws + WS_EPOCH), RLX_AGENT) + 1u;
  const int task = w < 4 ? w : 2 + (w >> 1), kc = task % 3, n = (task / 3) * 64 + lane;
  const bool doX = w < 4 || !(w & 1), doY = w < 4 || (w & 1);
  const int pc = min(d0 + kc, D_ - 1), po = pc * NS;
  const unsigned p_lar = lar[po + n], p_aim = aim[po + n], p_cr0 = cre[po + n], p_ci0 = cim[po + n], p_cr1 = cre[po + (n ^ 64)], p_ci1 = cim[po + (n ^ 64)], p_ldt = ldt[pc];
  const unsigned p_dk0 = dsk[min(d0, D_ - 1)], p_dk1 = dsk[min(d0 + 1, D_ - 1)], p_dk2 = dsk[min(d0 + 2, D_ - 1)];
  { const int rg_ = tid / 25;
    if (tid < 324) *(LAS u4*)LP(tid < 300 ? A_UREV + 16 + 2 * 2040 + rg_ * A_UC + 16 * (tid - 25 * rg_) : A_T0 + ((tid - 300) >> 3) * A_CH + 16 * ((tid - 300) & 7)) = (u4){0, 0, 0, 0}; }
  const unsigned tagA = tag << 16, tagB = (tag ^ (tag << 16)) & 0xffff0000u;
  const bool last = p1 == 106;
  const unsigned a[6] = {xr[0].x, xr[0].y, xr[0].z, last ? xr[1].y : xr[1].x, last ? xr[1].z : xr[1].y, last ? 0u : xr[1].z};
  const rsrc_t_b rs = __builtin_amdgcn_make_buffer_rsrc((void*)(ws + WS_X2), 0, 107 * 65536, 0x00020000);
  const int so = (8 * w + ps) * 65536 + bid * 128 + ii * 16;
  auto put = [&](unsigned h0, unsigned h1, unsigned h2, int off) {
    __builtin_amdgcn_raw_buffer_store_b128((u4){h0, __builtin_amdgcn_perm(tagA, h1, 0x07060100u), __builtin_amdgcn_alignbit(h2, h1, 16), __builtin_amdgcn_perm(tagB, h2, 0x07060302u)}, rs, so + off, 0, 16); };
  auto putx = [&]() { put(a[0], a[1], a[2], 32768); put(a[3], a[4], a[5], 64 * 65536 + 32768); };
  {
    const b2 one2 = __builtin_bit_cast(b2, 0x3f803f80u);
    float s[2] = {0.f, 0.f}, q[2] = {0.f, 0.f};
#pragma unroll
    for (int m = 0; m < 6; ++m) { const unsigned e = a[m]; const b2 v = __builtin_bit_cast(b2, e);
      s[m & 1] = __builtin_amdgcn_fdot2_f32_bf16(v, one2, s[m & 1], false); q[m & 1] = __builtin_amdgcn_fdot2_f32_bf16(v, v, q[m & 1], false); }
    float t = swap16add(swap32add(s[0] + s[1], q[0] + q[1])); t += dppf<0x128>(t);
    if ((lane & 24) == 0) *(LAS float*)LP(A_ST + (lane >> 5) * 256 + ii * 32 + 4 * w) = t;
    STAMP(1);
    LGBAR();
    STAMP(13);
    const f4 s0 = *(LAS f4*)LP(A_ST + ii * 32), s1 = *(LAS f4*)LP(A_ST + ii * 32 + 16), q0 = *(LAS f4*)LP(A_ST + 256 + ii * 32), q1 = *(LAS f4*)LP(A_ST + 256 + ii * 32 + 16);
    const f4 sv = s0 + s1, qv = q0 + q1;
    const f2 st = lnstat((sv.x + sv.y) + (sv.z + sv.w), (qv.x + qv.y) + (qv.z + qv.w), 1.f / D_);
    const float mr = -st.x * st.y;
#pragma unroll
    for (int m = 0; m < 2; ++m) put(lnh(a[3 * m], mr, st.y), lnh(a[3 * m + 1], mr, st.y), lnh(a[3 * m + 2], mr, st.y), m * (64 * 65536));
  }
  __builtin_amdgcn_s_setprio(0);
  STAMP(2);
  if (!conv) {
    putx();
    const int sj = slot >= 27 ? (slot - 27) * 8 + (bid & 7) : 40 + (cb - 214);
    if (sj < 27) {
      float acc[8] = {0, 0, 0, 0, 0, 0, 0, 0};
      const int r0 = tid >> 3, c8 = tid & 7;
#pragma unroll
      for (int i = 0; i < 10; ++i) {
        const u4 v = *(const u4*)(win + (size_t)(r0 + 64 * i) * F_ + sj * 64 + c8 * 8);
#pragma unroll
        for (int k = 0; k < 4; ++k) { acc[2 * k] += bflo(v[k]); acc[2 * k + 1] += bfhi(v[k]); }
      }
      LAS float* red = (LAS float*)S;
#pragma unroll
      for (int k = 0; k < 8; ++k) red[r0 * 65 + c8 * 8 + k] = acc[k];
      __syncthreads();
      if (tid < 64) { float t = 0.f; for (int r = 0; r < 64; ++r) t += red[r * 65 + tid]; __hip_atomic_store((GAS float*)(ws + WS_CS) + sj * 64 + tid, t, RLX_AGENT); }
    }
    if (sj >= 27 && w < 4) {
      const int p = (sj - 27) * 256 + tid;
      if (PW & 1) { const rsrc_t_b rz = MKB(ws + WS_ZS, 16 * 32768);
#pragma unroll 1
        for (int i = 0; i < 9; ++i) st16(rz, (i * 3840 + p) * 16, 0, (u4){~tag, ~tag, ~tag, ~tag}); }
    }
    if (slot >= 28 && w >= 4) {
      const int t4 = tid - 256;
      if (slot == 31) { unsigned clo, chi; CODE_ADDR(clo, chi, "kF"); const rsrc_t rc = {clo, chi & 0xffffu, F_CODE, 0x00020000u}; DMA4(rc, S + A_DUM, t4 * 128, 8192); }
      if (slot == 31) { const rsrc_t r0 = mkrs(ws, 24u << 20);
        DMA4(r0, S + A_DUM, t4 * TLBPF, 0); DMA4(r0, S + A_DUM, t4 * TLBPF, 256 * TLBPF); DMA4(mkrs(wsp, L_ * L_ * 2), S + A_DUM, t4 * TLBPF, 0); DMA4(mkrs(bsp, L_ * 2), S + A_DUM, t4 * TLBPF, 0);
        DMA4(mkrs(wout, FH * D_ * 2), S + A_DUM, t4 * TLBPF, 0); DMA4(mkrs(out, L_ * D_ * 2), S + A_DUM, t4 * TLBPF, 0); }
      {
        const rsrc_t_b rp = MKB(ws + WS_Z1 + (size_t)(bid - 224) * 110592, 110592);
#pragma unroll 1
        for (int i = 0; i < 27; ++i) st16(rp, (i * 256 + t4) * 16, 0, (u4){0u, 0u, 0u, 0u}); }
    }
    LGBAR();
  } else {
  {
    const float dt = rbf(__builtin_amdgcn_exp2f(bf2f(p_ldt) * 1.44269504088896340736f));
    const float ar = -rbf(__builtin_amdgcn_exp2f(bf2f(p_lar) * 1.44269504088896340736f)) * dt, ai = bf2f(p_aim) * dt;
    const float ar2 = ar * 1.44269504088896340736f, rev = ai * 0.15915494309189533577f;
    asm volatile("s_mov_b32 m0, %0\n\ts_nop 0" ::"s"(kc * A_TABCH + (task / 3) * 256) : "m0");
#define PUT(r, v) asm volatile("ds_write_addtid_b32 %0 offset:%1" ::"v"(pkh((v).x, (v).y)), "n"((r) * A_ROW))
    if (doY) {
      const f2 E8 = cexpk(ar2, rev, 8.f), E64 = cexpk(ar2, rev, 64.f), E16 = csq(E8), E32 = csq(E16), E128 = csq(E64);
      f2 Y[16];
      Y[0] = (f2){1.f, 0.f}; Y[1] = E8; Y[2] = E16; Y[3] = cmul(Y[1], E16); Y[4] = E32;
#pragma unroll
      for (int j = 1; j < 4; ++j) Y[4 + j] = cmul(Y[j], E32);
      Y[8] = E64;
#pragma unroll
      for (int j = 1; j < 8; ++j) Y[8 + j] = cmul(Y[j], E64);
#pragma unroll
      for (int j = 0; j < 16; ++j) PUT(27 + j, Y[j]);
      Y[0] = E128;
#pragma unroll
      for (int j = 1; j < 16; ++j) Y[j] = cmul(Y[j], E128);
#pragma unroll
      for (int j = 0; j < 16; ++j) PUT(43 + j, Y[j]);
    }
    if (doX) {
      float sa = fabsf(bf2f(p_cr0)) + fabsf(bf2f(p_ci0)) + fabsf(bf2f(p_cr1)) + fabsf(bf2f(p_ci1));
      sa = swap16add(rowsum16(sa)); sa = swap32add(sa, sa) * dt;
      int e = 13 - (int)((__float_as_uint(sa) >> 23) & 0xff) + 127; e = max(-60, min(60, e));
      const float sc = __uint_as_float((unsigned)(e + 127) << 23);
      if (w < 3) LDSW(A_MISC + 4 * kc) = (unsigned)(128 - e) << 23;
      const f2 C = {bf2f(p_cr0) * dt * sc, -bf2f(p_ci0) * dt * sc};
      const f2 E1 = cexpk(ar2, -rev, 1.f), E256 = cexpk(ar2, -rev, 256.f), E2 = csq(E1), E512 = csq(E256), E4 = csq(E2), E1024 = csq(E512), E8 = csq(E4);
      const f2 X1 = cmul(C, E1), X2 = cmul(C, E2), X4 = cmul(C, E4), X8 = cmul(C, E8), X3 = cmul(X1, E2), X5 = cmul(X1, E4), X6 = cmul(X2, E4), X7 = cmul(X3, E4);
      PUT(0, C); PUT(1, X1); PUT(2, X2); PUT(3, X3); PUT(4, X4); PUT(5, X5); PUT(6, X6); PUT(7, X7);
      const f2 X256 = cmul(C, E256), X258 = cmul(X2, E256), X260 = cmul(X4, E256), X262 = cmul(X6, E256), X264 = cmul(X8, E256);
      const f2 X512 = cmul(C, E512), X516 = cmul(X4, E512), X520 = cmul(X8, E512), X768 = cmul(X256, E512), X772 = cmul(X260, E512), X776 = cmul(X264, E512);
      PUT(8, X256); PUT(9, X258); PUT(10, X260); PUT(11, X262); PUT(12, X264);
      PUT(13, X512); PUT(14, X516); PUT(15, X520); PUT(16, X768); PUT(17, X772); PUT(18, X776);
      const f2 t0 = cmul(C, E1024), t1 = cmul(X8, E1024), t2 = cmul(X256, E1024), t3 = cmul(X264, E1024), t4 = cmul(X512, E1024), t5 = cmul(X520, E1024), t6 = cmul(X768, E1024), t7 = cmul(X776, E1024);
      PUT(19, t0); PUT(20, t1); PUT(21, t2); PUT(22, t3); PUT(23, t4); PUT(24, t5); PUT(25, t6); PUT(26, t7);
    }
  }
  STAMP(3);
  const rsrc_t_b rg = __builtin_amdgcn_make_buffer_rsrc((void*)(ws + WS_X2 + (size_t)(cb >> 1) * 65536), 0, 65536, 0x00020000);
  u4 gv[4], gp;
  auto fetch = [&](int off) { int vo = off + w * 4096 + lane * 16; asm volatile("" : "+v"(vo));
#pragma unroll
    for (int k = 0; k < 4; ++k) gv[k] = __builtin_amdgcn_raw_buffer_load_b128(rg, vo + 1024 * k, 0, 16);
    if (off == 0) gp = __builtin_amdgcn_raw_buffer_load_b128(rg, vo + (lane ? 3056 : -16), 0, 16); };
  u2 uk[4];
  auto await = [&](int off) {
    for (unsigned spins = 0;; ++spins) {
      unsigned bad = 0;
#pragma unroll
      for (int k = 0; k < 4; ++k) bad |= (gv[k].y ^ tagA) | (gv[k].w ^ tagB);
      if (off == 0 && tid != 0) bad |= (gp.y ^ tagA) | (gp.w ^ tagB);
      if (__all((bad >> 16) == 0) || spins > (1u << 18)) break;
      asm volatile("s_sleep 1" ::: "memory");
      fetch(off);
    } };
  auto uprep = [&]() {
    await(0);
    STAMP(12);
#pragma unroll
    for (int k = 0; k < 4; ++k) uk[k] = (cb & 1) ? gv[k].zw : gv[k].xy;
    const u2 up = (cb & 1) ? gp.zw : gp.xy;
    typedef u2 u2a __attribute__((aligned(4)));
    LAS char* p = LP(A_UREV + 16 + 2 * 2040 - 512 * w - 8 * lane);
#pragma unroll
    for (int c = 0; c < 3; ++c) {
      const unsigned sl = c == 1 ? 0x07060302u : 0x05040100u;
      const unsigned v0 = c < 2 ? uk[0].x : uk[0].y, v1 = c < 2 ? uk[1].x : uk[1].y, v2 = c < 2 ? uk[2].x : uk[2].y, v3 = c < 2 ? uk[3].x : uk[3].y;
      const unsigned vp = c < 2 ? up.x : up.y;
      const unsigned r32 = __builtin_amdgcn_perm(v2, v3, sl), r10 = __builtin_amdgcn_perm(v0, v1, sl), r21 = __builtin_amdgcn_perm(v1, v2, sl), r0p = __builtin_amdgcn_perm(vp, v0, sl);
      LAS char* q = p + c * A_UCH;
      *(LAS u2*)(q + 3 * A_UC + 8) = (u2){r32, r10};
      *(LAS u2a*)(q + A_UC + 4) = (u2){r32, r10};
      *(LAS u2a*)(q + 4) = (u2){r21, r0p};
      *(LAS u2*)(q + 2 * A_UC + 8) = (u2){r21, r0p};
    }
    putx(); };
  fetch(0);
  const int j = lane & 31, hh = lane >> 5;
  int ta_ = w * A_TABCH + (j - max(0, min(j - 11, 4)) - (j >= 20)) * A_ROW + hh * 16, tb_ = w * A_TABCH + (27 + j) * A_ROW + hh * 16;
  asm volatile("" : "+v"(ta_), "+v"(tb_));
  LGBAR();
  STAMP(4);
  if (w < 3) {
    LAS char* ta = LP(ta_); LAS char* tb = LP(tb_);
    h8 fa[16], fb[16];
#pragma unroll
    for (int ks = 0; ks < 16; ++ks) { fa[ks] = *(LAS h8*)(ta + ks * 32); fb[ks] = *(LAS h8*)(tb + ks * 32); }
    __builtin_amdgcn_sched_barrier(0);
    f16v acc = {}, ac2 = {};
#pragma unroll
    for (int ks = 0; ks < 16; ks += 2) { acc = __builtin_amdgcn_mfma_f32_32x32x16_f16(fa[ks], fb[ks], acc, 0, 0, 0); ac2 = __builtin_amdgcn_mfma_f32_32x32x16_f16(fa[ks + 1], fb[ks + 1], ac2, 0, 0, 0); }
    acc += ac2;
    LAS char* kb = LP(A_T0 + w * A_CH + 128);
    *(LAS u2*)(kb + 2 * (8 * j + 4 * hh)) = (u2){pkh(acc[0], acc[1]), pkh(acc[2], acc[3])};
    if (hh == 0) *(LAS u4*)(kb + 2 * (256 + 8 * j)) = (u4){pkh(acc[4], acc[4]), pkh(acc[5], acc[6]), pkh(acc[6], acc[6]), pkh(acc[7], acc[8])};
    *(LAS u4*)(kb + 2 * (512 + 256 * hh + 8 * j)) = (u4){pkh(acc[9], acc[9]), pkh(acc[9], acc[10]), pkh(acc[10], acc[10]), pkh(acc[11], acc[11])};
    { const unsigned aa = pkh(acc[12], acc[12]), bb = pkh(acc[13], acc[13]), m = pkh((j & 1) ? acc[13] : acc[12], acc[13]);
      *(LAS u4*)(kb + 2 * (1024 + 512 * hh + 8 * j)) = (u4){aa, aa, m, bb}; }
    { const unsigned aa = pkh(acc[14], acc[14]), bb = pkh(acc[15], acc[15]), m = pkh((j & 1) ? acc[15] : acc[14], acc[15]);
      *(LAS u4*)(kb + 2 * (1280 + 512 * hh + 8 * j)) = (u4){aa, aa, m, bb}; }

    STAMP(5);
  }
  uprep();
  STAMP(7);
  LGBAR();
  STAMP(8);
  float rr[3][4];
  const f4 ysc = *(LAS f4*)LP(A_MISC);
  {
    typedef h8 h8a __attribute__((aligned(8)));
    const int g = lane >> 4;
    const int koff = A_T0 + 8 + 16 * g + 8 * (lane & 15);
    const int uoff = A_UREV + 16 + (lane & 3) * A_UC + 384 - 128 * ((lane >> 2) & 3) + 16 * g;
    h8 A[2][8], B[2][8];
    f4 acc[8];
    auto load = [&](int u, int b) {
      LAS char* pk = LP(koff + (u >> 3) * A_CH + 64 * (u & 7));
      LAS char* pu = LP(uoff + (u >> 3) * A_UCH + 64 * (u & 7));
#pragma unroll
      for (int m = 0; m < 8; ++m) { A[b][m] = *(LAS h8*)(pu + 512 * (7 - m)); B[b][m] = *(LAS h8a*)(pk + 512 * m); } };
    const int ef = w + (w >= 2) + (w >= 5);
    auto dump = [&](int e) {
#pragma unroll
      for (int q = 0; q < 8; ++q) *(LAS f4*)(LP(A_EX + e * 8192 + lane * 16) + q * 1024) = acc[q]; };
    load(3 * w, 0);
#pragma unroll
    for (int q = 0; q < 8; ++q) acc[q] = (f4){0, 0, 0, 0};
#pragma unroll
    for (int i = 0; i < 3; ++i) {
      __builtin_amdgcn_sched_barrier(0);
      if (i == 1) { fetch(32768); __builtin_amdgcn_sched_barrier(0); }
      if (i == 2) {
    await(32768);
    if (tid == 0 && bid == 0) __hip_atomic_store((GAS unsigned*)(ws + WS_EPOCH), tag, RLX_AGENT);
#pragma unroll
    for (int k = 0; k < 4; ++k) { const u2 v = (cb & 1) ? gv[k].zw : gv[k].xy;
      rr[0][k] = __builtin_fmaf(hlo(uk[k].x), bf2f(p_dk0), bflo(v.x)); rr[1][k] = __builtin_fmaf(hhi(uk[k].x), bf2f(p_dk1), bfhi(v.x)); rr[2][k] = __builtin_fmaf(hlo(uk[k].y), bf2f(p_dk2), bflo(v.y)); }
      __builtin_amdgcn_sched_barrier(0); }
      if (i < 2) load(3 * w + i + 1, (i & 1) ^ 1);
#pragma unroll
      for (int m = 0; m < 8; ++m)
#pragma unroll
        for (int q = m; q < 8; ++q) acc[q] = __builtin_amdgcn_mfma_f32_16x16x32_f16(A[i & 1][q - m], B[i & 1][m], acc[q], 0, 0, 0);
      if (i < 2) {
#pragma unroll
        for (int k = 0; k < 16; ++k) { __builtin_amdgcn_sched_group_barrier(0x008, 2, 0); __builtin_amdgcn_sched_group_barrier(0x100, 1, 0); } }
      __builtin_amdgcn_sched_barrier(0);
      if ((i == 1 && w == 2) || (i == 0 && w == 5)) { dump(ef - 1);
#pragma unroll
        for (int q = 0; q < 8; ++q) acc[q] = (f4){0, 0, 0, 0}; }
    }
    dump(ef);
  }
  STAMP(9);
  LGBAR();
  STAMP(10);
  { f4 e[10];
#pragma unroll
    for (int i = 0; i < 10; ++i) e[i] = *(LAS f4*)(LP(A_EX + w * 1024 + lane * 16) + i * 8192);
    const f4 y[3] = {e[0] + e[1] + e[2], (e[3] + e[4]) + (e[5] + e[6]), e[7] + e[8] + e[9]};
    LGBAR();
    u2 hv[3];
#pragma unroll
    for (int c = 0; c < 3; ++c) { const float ys = c == 0 ? ysc.x : c == 1 ? ysc.y : ysc.z;
      hv[c] = (u2){pkbf(__builtin_fmaf(y[c].x, ys, rr[c][0]), __builtin_fmaf(y[c].y, ys, rr[c][1])), pkbf(__builtin_fmaf(y[c].z, ys, rr[c][2]), __builtin_fmaf(y[c].w, ys, rr[c][3]))}; }
    const bool odd = lane & 1;
    const u2 sn = odd ? hv[0] : hv[1];
    const u2 rc = {(unsigned)__builtin_amdgcn_update_dpp(0, (int)sn.x, 0xB1, 0xf, 0xf, true), (unsigned)__builtin_amdgcn_update_dpp(0, (int)sn.y, 0xB1, 0xf, 0xf, true)};
    const u2 r2 = {(unsigned)__builtin_amdgcn_update_dpp(0, (int)hv[2].x, 0xB1, 0xf, 0xf, true), (unsigned)__builtin_amdgcn_update_dpp(0, (int)hv[2].y, 0xB1, 0xf, 0xf, true)};
    const rsrc_t_b rh = MKB(ws + WS_HT, D_ * L_ * 2);
    const int ho = d0 * 4096 + 512 * w + 16 * (lane >> 1);
    st16(rh, ho + (odd ? 4096 : 0), 0, odd ? (u4){rc.x, rc.y, hv[1].x, hv[1].y} : (u4){hv[0].x, hv[0].y, rc.x, rc.y});
    st16(rh, odd ? 0x7f000000 : ho + 8192, 0, (u4){hv[2].x, hv[2].y, r2.x, r2.y});
  }
  }
  STAMP(11); STAMP_OUT(ws, 0);
  }

  constexpr int B_R = 8, B_SLOT = 16384, B_ST = 153600, B_CS = B_ST + 2048;
  constexpr int C_R = 5, C_SLOT = 30720;
  constexpr int D_H = 135 * 1024;
#define CDEEP 0
#define ZLATE 0
#define EARLY 1
#define SPF 3
#define C_XS 8
#define D_XS 1
  const int v = w & 3, c = lane & 15, g = lane >> 4, qq = (lane >> 2) & 3, pp = lane & 3;
  const bool gemm1 = slot < 28;
  const int PB = 4 * (xcd >> 1) + min(slot, 27) / 7, G = 7 * (xcd & 1) + slot % 7;
  const bool spec = G == 13;
  const int u2b = spec ? 0 : 2 + 4 * G, u1b = spec ? 52 : 4 * G, nv = spec ? 2 : 4;
  const int hf = xcd & 1, PC = 4 * (xcd >> 1) + (slot >> 3), Cg = slot & 7;
  const int tp = 4 * xcd + (slot >> 3), dg = slot & 7;
  auto relayout = [&]() {
    const int sj = (slot - 28) * 8 + xcd;
    if (sj < 27) {
#pragma unroll
      for (int i = 0; i < 5; ++i) *(LAS u4*)(S + 16 * (tid + 512 * i)) = *(const u4*)((const char*)wout + sj * 40960 + 16 * (tid + 512 * i));
      LGBAR();
      const rsrc_t_b ro = MKB(ws + WS_WO, 27 * 40 * 1024);
#pragma unroll
      for (int i = 0; i < 5; ++i) { const int U = w + 8 * i; LAS char* p = S + (8 * g + qq) * 1280 + 32 * U + 8 * pp;
        st16(ro, (sj * 40 + U) * 1024 + lane * 16, 0, __builtin_bit_cast(u4, trd(p, p + 4 * 1280))); }
    }
    { const rsrc_t_b rp = MKB(ws + WS_P + (size_t)sj * 225280, 225280);
#pragma unroll 1
      for (int i = 0; i < 28; ++i) st16(rp, (i * 512 + tid) * 16, 0, (u4){~0u, ~0u, ~0u, ~0u}); }
    asm volatile("s_waitcnt vmcnt(0) lgkmcnt(0)\n\ts_barrier" ::: "memory"); };
  if constexpr (P0 > 0) asm volatile("" :: "s"(P0 == 1 ? win : wsp), "s"(ws));
  if (__builtin_expect(w >= 4, 1)) {
    if constexpr (P1 == 0) return;
    if constexpr (P0 <= 1) { STAMP_DECL STAMP(0);
    if (gemm1) {
      const rsrc_t rA = mkrs(ws + WS_HT, D_ * L_ * 2), rB = mkrs(win, D_ * F_ * 2);
      const int r8 = (lane >> 2) & 7, sw = (2 * v + (r8 >> 2)) & 3, chl = 4 * (lane >> 5) + ((lane & 3) ^ sw);
      const int vA = (8 * v + r8) * 4096 + PB * 256 + chl * 16;
      const int vB2 = (8 * v + r8) * 3456 + (FH + 16 * u2b) * 2 + (spec ? (chl & 3) : chl) * 16, vB1 = (8 * v + r8) * 3456 + 32 * u1b + chl * 16;
      auto issA = [&](int ks) { LAS char* d = S + (ks & (B_R - 1)) * B_SLOT + 2048 * v; DMA16S(rA, d, vA, ks * 131072); DMA16S(rA, d + 1024, vA + 128, ks * 131072); };
      auto issB = [&](int ks) { LAS char* d = S + (ks & (B_R - 1)) * B_SLOT + 2048 * v; DMA16(rB, d + 8192, vB2, ks * 110592); DMA16(rB, d + 9216, vB1, ks * 110592); };
      STAMP(5);
#pragma unroll 1
      for (int ks = 0; ks < 4; ++ks) { issA(ks); issB(ks); }
      STAMP(1);
      VMBAR(8); STAMP(3);
      issA(4); issB(4); issA(5); issB(5); VMBAR(8);
      issA(6); issB(6); issA(7); issB(7);
      { const rsrc_t rC = mkrs(ws + WS_CS, F_ * 4); DMA16S(rC, S + B_CS, ((lane & 16) ? 64 * u1b : 4 * FH + 64 * u2b) + 16 * c, 0); }
      issA(8); issB(8); issA(9); issB(9); VMBAR(17);
#pragma unroll 1
      for (int ks = 10; ks < 20; ks += 2) { issA(ks); issB(ks); issA(ks + 1); issB(ks + 1); VMBAR(16); }
      touch_wsp(); VMBAR(9); VMBAR(1); BAR();
    } else {
      touch_wsp();
      relayout();
    }
    STAMP(2); STAMP_OUT(ws, 1); }
    if constexpr (P1 == 1) return;
    if constexpr (P0 <= 2) { STAMP_DECL STAMP(0);
    const rsrc_t rW = mkrs(wsp, L_ * L_ * 2), rZ = mkrs(ws + WS_Z2, 64 * 54 * 1024);
    const int r8 = lane >> 3;
    const int vW0 = (128 * PC + 32 * v + r8) * 4096 + hf * 2048 + ((lane & 7) ^ (r8 >> 1)) * 16, vW1 = (128 * PC + 32 * v + 8 + r8) * 4096 + hf * 2048 + ((lane & 7) ^ (4 + (r8 >> 1))) * 16;
    const int vZ = lane * 16;
    int zo[4];
#pragma unroll
    for (int j = 0; j < 4; ++j) { const int n = v + 4 * j; zo[j] = ((32 * hf + n / 7) * 54 + 7 * Cg + n % 7) * 1024; }
    auto issW = [&](int s, int so) { LAS char* d = S + so + 4096 * v;
      DMA16(rW, d, vW0, s * 128); DMA16(rW, d + 1024, vW1, s * 128); DMA16(rW, d + 2048, vW0, s * 128 + 65536); DMA16(rW, d + 3072, vW1, s * 128 + 65536); };
    auto issZ = [&](int s, int so) { LAS char* d = S + so + 16384 + 1024 * v;
      DMA16S(rZ, d, vZ, zo[0] + s * 110592); DMA16S(rZ, d + 4096, vZ, zo[1] + s * 110592); DMA16S(rZ, d + 8192, vZ, zo[2] + s * 110592);
      if (v < 2) DMA16S(rZ, d + 12288, vZ, zo[3] + s * 110592); };
#define CWAIT(nz, nw) do { if (v < 2) VMBAR(4 * (nz) + 4 * (nw)); else VMBAR(3 * (nz) + 4 * (nw)); } while (0)
    STAMP(5);
    issW(0, 0); issZ(0, 0); issW(1, C_SLOT); issZ(1, C_SLOT);
    STAMP(1);
    CWAIT(1, 1); STAMP(3);
    issW(2, 2 * C_SLOT); issZ(2, 2 * C_SLOT); CWAIT(1, 1);
    issW(3, 3 * C_SLOT); issZ(3, 3 * C_SLOT); issW(4, 4 * C_SLOT); issZ(4, 4 * C_SLOT);
    int so = 0;
    CWAIT(2, 2);
#pragma unroll 1
    for (int st = 5; st < 16; ++st) {
      issW(st, so); issZ(st, so); so = so == (C_R - 1) * C_SLOT ? 0 : so + C_SLOT; CWAIT(2, 2); }
    CWAIT(1, 1); VMBAR(0); BAR();
    STAMP(2); STAMP_OUT(ws, 2); }
    if constexpr (P1 == 2) return;
    { STAMP_DECL STAMP(0);
    const rsrc_t rH = mkrs(ws + WS_HT, D_ * L_ * 2), rO = mkrs(ws + WS_WO, 27 * 40 * 1024);
#pragma unroll 1
    for (int ks = v; ks < 27; ks += 4) {
#pragma unroll
      for (int u = 0; u < 5; ++u) DMA16S(rO, S + 1024 * (5 * ks + u), lane * 16, (ks * 40 + 5 * dg + u) * 1024);
      asm volatile("s_waitcnt vmcnt(%0)" ::"n"(D_PACE) : "memory"); }
    if (v == 3) {
#pragma unroll 1
      for (int n = 0; n < 10; ++n) DMA16S(rH, S + D_H + 1024 * n, (lane >> 3) * 4096 + (lane & 7) * 16, (80 * dg + 8 * n) * 4096 + tp * 128); }
    VMBAR(0);
    STAMP(1); STAMP_OUT(ws, 3); }
    return;
  }
  if constexpr (P1 == 0) return;
  if constexpr (P0 <= 1) { STAMP_DECL STAMP(0);
  if ((SPF & 1) && w == 0) { unsigned clo, chi; CODE_ADDR(clo, chi, "k1"); const rsrc_t rc = {clo, chi & 0xffffu, 11776u, 0x00020000u};
    DMA4(rc, S + F_DUM, lane * 128, 0); DMA4(rc, S + F_DUM, lane * 128, 8192); }
  if (gemm1) {
  tag = __hip_atomic_load((GAS unsigned*)(ws + WS_EPOCH), RLX_AGENT);
  int av[4], bv[4];
#pragma unroll
  for (int k = 0; k < 4; ++k) { const int par = k >> 1, hi = k & 1;
    bv[k] = 2048 * g + 64 * (4 * hi + qq) + 16 * ((2 * par + (pp >> 1)) ^ ((2 * g + hi) & 3)) + 8 * (pp & 1); av[k] = bv[k] + 512 * w; }
  b8 fa[2][2], fb[2][8];
  auto rd = [&](int j) {
    const int s = (j & 3) * B_SLOT, b = j & 1;
#pragma unroll
    for (int q = 0; q < 2; ++q) fa[b][q] = trd(LP(av[2 * q]) + s, LP(av[2 * q + 1]) + s);
#pragma unroll
    for (int u = 0; u < 8; ++u) fb[b][u] = trd(LP(bv[2 * (u & 1)]) + s + 8192 + 512 * (u >> 1), LP(bv[2 * (u & 1) + 1]) + s + 8192 + 512 * (u >> 1)); };
  f4 a2[4][2], a1[4][2], aS[2], aM[2];
#pragma unroll
  for (int q = 0; q < 2; ++q) { aS[q] = aM[q] = (f4){0, 0, 0, 0};
#pragma unroll
    for (int u = 0; u < 4; ++u) a2[u][q] = a1[u][q] = (f4){0, 0, 0, 0}; }
  const b8 ones = __builtin_bit_cast(b8, (u4){0x3f803f80u, 0x3f803f80u, 0x3f803f80u, 0x3f803f80u});
  STAMP(1);
  BAR();
  STAMP(2);
  rd(0);
#pragma unroll 1
  for (int it = 0; it < 5; ++it) {
#pragma unroll
    for (int j = 0; j < 4; ++j) {
      if (j == 3) {
#pragma unroll
        for (int k = 0; k < 4; ++k) { av[k] ^= 65536; bv[k] ^= 65536; } }
      __builtin_amdgcn_sched_barrier(0);
      if (j & 1) LGBAR();
      rd(j + 1);
      const int b = j & 1;
#pragma unroll
      for (int u = 0; u < 4; ++u)
#pragma unroll
        for (int q = 0; q < 2; ++q) { a2[u][q] = MFMA(fa[b][q], fb[b][u], a2[u][q]); a1[u][q] = MFMA(fb[b][4 + u], fa[b][q], a1[u][q]); }
#pragma unroll
      for (int q = 0; q < 2; ++q) { aS[q] = MFMA(fa[b][q], fa[b][q], aS[q]); aM[q] = MFMA(fa[b][q], ones, aM[q]); }
#pragma unroll
      for (int i = 0; i < 10; ++i) { __builtin_amdgcn_sched_group_barrier(0x008, 1, 0); __builtin_amdgcn_sched_group_barrier(0x100, 2, 0); }
    }
  }
  __builtin_amdgcn_sched_barrier(0);
  STAMP(3);
  float cs2[4]; f4 cs1[4];
#pragma unroll
  for (int u = 0; u < 4; ++u) { cs2[u] = *(LAS float*)(S + B_CS + 64 * u + 4 * c); cs1[u] = *(LAS f4*)(S + B_CS + 256 + 64 * u + 16 * g); }
  LAS char* st = S + B_ST + 256 * w;
#pragma unroll
  for (int q = 0; q < 2; ++q) {
    const int r = c & 3;
    const float ss = sel4(r, aS[q][0], aS[q][1], aS[q][2], aS[q][3]), sm = sel4(r, aM[q][0], aM[q][1], aM[q][2], aM[q][3]);
    const f2 sv = lnstat(sm, ss, 1.f / D_);
    if ((c >> 2) == g) *(LAS f2*)(st + 8 * (16 * q + c)) = sv;
  }
  unsigned zp[4][2][2];
  const rsrc_t_b rzs = MKB(ws + WS_ZS + (size_t)PB * 32768, 32768);
#pragma unroll
  for (int q = 0; q < 2; ++q) {
    const f4 s01 = *(LAS f4*)(st + 8 * (16 * q + 4 * g)), s23 = *(LAS f4*)(st + 8 * (16 * q + 4 * g) + 16);
    const f2 m01 = {-s01.x, -s01.z}, r01 = {s01.y, s01.w}, m23 = {-s23.x, -s23.z}, r23 = {s23.y, s23.w};
    f2 sm01 = {0, 0}, sm23 = {0, 0}, sq01 = {0, 0}, sq23 = {0, 0};
#pragma unroll
    for (int u = 0; u < 4; ++u) {
      const f2 cc = {cs2[u], cs2[u]};
      const f2 x01 = gelu2(__builtin_elementwise_fma(m01, cc, a2[u][q].xy) * r01), x23 = gelu2(__builtin_elementwise_fma(m23, cc, a2[u][q].zw) * r23);
      zp[u][q][0] = pkbf(x01.x, x01.y); zp[u][q][1] = pkbf(x23.x, x23.y);
      if (u < 2 || !spec) {
        const f2 y01 = {bflo(zp[u][q][0]), bfhi(zp[u][q][0])}, y23 = {bflo(zp[u][q][1]), bfhi(zp[u][q][1])};
        sm01 += y01; sm23 += y23; sq01 = __builtin_elementwise_fma(y01, y01, sq01); sq23 = __builtin_elementwise_fma(y23, y23, sq23); }
    }
    const float t0 = rowsum16(sm01.x), t1 = rowsum16(sm01.y), t2 = rowsum16(sm23.x), t3 = rowsum16(sm23.y);
    const float v0 = rowsum16(sq01.x), v1 = rowsum16(sq01.y), v2 = rowsum16(sq23.x), v3 = rowsum16(sq23.y);
    const float ts = sel4(c, t0, t1, t2, t3), vs = sel4(c, v0, v1, v2, v3);
    __builtin_amdgcn_raw_buffer_store_b128((u4){__float_as_uint(ts), tag, __float_as_uint(vs), tag}, rzs, c < 4 ? (G * 128 + 32 * w + 16 * q + 4 * g + c) * 16 : 0x7f000000, 0, 16);
  }
  STAMP(4);
  u4 z1p[4];
  { const f2 s0 = *(LAS f2*)(st + 8 * c), s1 = *(LAS f2*)(st + 8 * (16 + c));
#pragma unroll
    for (int u = 0; u < 4; ++u) {
      const f2 a = gelu2(__builtin_elementwise_fma(-s0.xx, cs1[u].xy, a1[u][0].xy) * s0.yy), b = gelu2(__builtin_elementwise_fma(-s0.xx, cs1[u].zw, a1[u][0].zw) * s0.yy);
      const f2 d = gelu2(__builtin_elementwise_fma(-s1.xx, cs1[u].xy, a1[u][1].xy) * s1.yy), e = gelu2(__builtin_elementwise_fma(-s1.xx, cs1[u].zw, a1[u][1].zw) * s1.yy);
      z1p[u] = (u4){pkbf(a.x, a.y), pkbf(b.x, b.y), pkbf(d.x, d.y), pkbf(e.x, e.y)};
      if (!ZLATE && u < nv) st16(MKB(ws + WS_Z1, 64 * 54 * 1024), lane * 16, ((4 * PB + w) * 54 + u1b + u) * 1024, z1p[u]);
    } }
  STAMP(5);
  { u4 gv[7];
    for (unsigned spins = 0;; ++spins) {
      bool ok = true;
      int vo = ((lane >> 5) * 7 * 128 + 32 * w + (lane & 31)) * 16; asm volatile("" : "+v"(vo));
#pragma unroll
      for (int i = 0; i < 7; ++i) gv[i] = __builtin_amdgcn_raw_buffer_load_b128(rzs, vo, i * 2048, 16);
      if (ZLATE && spins == 0) {
        __builtin_amdgcn_sched_barrier(0);
#pragma unroll
        for (int u = 0; u < 4; ++u) if (u < nv) st16(MKB(ws + WS_Z1, 64 * 54 * 1024), lane * 16, ((4 * PB + w) * 54 + u1b + u) * 1024, z1p[u]);
        __builtin_amdgcn_sched_barrier(0); }
#pragma unroll
      for (int i = 0; i < 7; ++i) ok &= gv[i].y == tag && gv[i].w == tag;
      if (__all(ok) || spins > (1u << 18)) break;
      asm volatile("s_sleep 1" ::: "memory");
    }
    float sm = 0.f, sq = 0.f;
#pragma unroll
    for (int i = 0; i < 7; ++i) { sm += __uint_as_float(gv[i].x); sq += __uint_as_float(gv[i].z); }
    const float sm2 = __shfl_xor(sm, 32), sq2 = __shfl_xor(sq, 32);
    const f2 sv = lane < 32 ? lnstat(sm + sm2, sq + sq2, 1.f / FH) : lnstat(sm2 + sm, sq2 + sq, 1.f / FH);
    if (lane < 32) *(LAS f2*)(st + 1024 + 8 * lane) = sv; }
  STAMP(6);
#pragma unroll
  for (int q = 0; q < 2; ++q) {
    const f4 s01 = *(LAS f4*)(st + 1024 + 8 * (16 * q + 4 * g)), s23 = *(LAS f4*)(st + 1024 + 8 * (16 * q + 4 * g) + 16);
    const f2 m01 = {s01.x, s01.z}, r01 = {s01.y, s01.w}, m23 = {s23.x, s23.z}, r23 = {s23.y, s23.w};
    u2 zn[4];
#pragma unroll
    for (int u = 0; u < 4; ++u) {
      const f2 a = ((f2){bflo(zp[u][q][0]), bfhi(zp[u][q][0])} - m01) * r01, b = ((f2){bflo(zp[u][q][1]), bfhi(zp[u][q][1])} - m23) * r23;
      zn[u] = (u2){pkbf(a.x, a.y), pkbf(b.x, b.y)}; }
#pragma unroll
    for (int u = 0; u < 4; u += 2) { const u4 t = pair16(zn[u], zn[u + 1]);
      if (u < nv) st16(MKB(ws + WS_Z2, 64 * 54 * 1024), (g & 1) * 1024 + ((2 * q + (g >> 1)) * 16 + c) * 16, ((4 * PB + w) * 54 + u2b + u) * 1024, t); }
  }
  } else relayout();
  STAMP(7); STAMP_OUT(ws, 1); }
  if constexpr (P1 == 1) return;
  if constexpr (P0 <= 2) { STAMP_DECL STAMP(0);
  if ((SPF & 2) && w == 0) { unsigned clo, chi; CODE_ADDR(clo, chi, "k2"); const rsrc_t rc = {clo, chi & 0xffffu, 13312u, 0x00020000u};
    DMA4(rc, S + F_DUM, lane * 128, 0); DMA4(rc, S + F_DUM, lane * 128, 8192); }
  const int ra0 = (32 * w + c) * 128 + 16 * (g ^ (c >> 1)), ra1 = (32 * w + c) * 128 + 16 * ((4 + g) ^ (c >> 1));
  int so = 0;
  b8 fw[2][2], fz[2][7];
  auto rd = [&](int b) {
#pragma unroll
    for (int q = 0; q < 2; ++q) fw[b][q] = *(LAS b8*)(LP(so + (b ? ra1 : ra0)) + 2048 * q);
#pragma unroll
    for (int u = 0; u < 7; ++u) fz[b][u] = *(LAS b8*)(LP(so + lane * 16) + 16384 + (7 * b + u) * 1024); };
  f4 acc[7][2];
#pragma unroll
  for (int u = 0; u < 7; ++u) acc[u][0] = acc[u][1] = (f4){0, 0, 0, 0};
  STAMP(1);
  BAR();
  STAMP(2);
  rd(0);
  auto stage = [&]() {
#pragma unroll
    for (int b = 0; b < 2; ++b) {
      if (b) so = so == (C_R - 1) * C_SLOT ? 0 : so + C_SLOT;
      __builtin_amdgcn_sched_barrier(0);
      if (b) LGBAR();
      rd(b ^ 1);
#pragma unroll
      for (int u = 0; u < 7; ++u)
#pragma unroll
        for (int q = 0; q < 2; ++q) acc[u][q] = MFMA(fz[b][u], fw[b][q], acc[u][q]);
#pragma unroll
      for (int i = 0; i < 9; ++i) { __builtin_amdgcn_sched_group_barrier(0x008, 1, 0); __builtin_amdgcn_sched_group_barrier(0x100, 1, 0); }
    } };
#pragma unroll 1
  for (int st = 0; st < 6; ++st) stage();
#pragma unroll 1
  for (int st = 6; st < 12; ++st) stage();
  __builtin_amdgcn_sched_barrier(0);
  u4 z1[7]; unsigned bsr[2];
  const bool keep = (w >> 1) == hf;
#pragma unroll
  for (int u = 0; u < 7; ++u) z1[u] = __builtin_amdgcn_raw_buffer_load_b128(MKB(ws + WS_Z1, 64 * 54 * 1024), keep ? lane * 16 : 0x7f000000, ((4 * PC + w) * 54 + min(7 * Cg + u, 53)) * 1024, 16);
#pragma unroll
  for (int q = 0; q < 2; ++q) bsr[q] = bsp[128 * PC + 32 * w + 16 * q + c];
#pragma unroll 1
  for (int st = 12; st < 16; ++st) stage();
  __builtin_amdgcn_sched_barrier(0);
  STAMP(3);
  const rsrc_t_b rX = MKB(ws + WS_P + 128 * 27 * 1024, 128 * 4 * 7 * 1024);
  const int xo = ((PC * 8 + Cg) * 4 + w) * 7168 + lane * 16;
  if (!keep) {
#pragma unroll
    for (int u = 0; u < 7; ++u) st16(rX, xo + 1024 * u, 0, (u4){pkbf(acc[u][0].x, acc[u][0].y), min(pkbf(acc[u][0].z, acc[u][0].w), 0xfffffffeu), pkbf(acc[u][1].x, acc[u][1].y), min(pkbf(acc[u][1].z, acc[u][1].w), 0xfffffffeu)});
  } else {
    u4 pr[7];
    asm volatile("s_sleep %0" ::"n"(C_XS) : "memory");
    for (unsigned spins = 0;; ++spins) {
      int vo = xo; asm volatile("" : "+v"(vo));
      unsigned m = 0;
#pragma unroll
      for (int u = 0; u < 7; ++u) pr[u] = __builtin_amdgcn_raw_buffer_load_b128(rX, vo + 1024 * u, 0, 16);
#pragma unroll
      for (int u = 0; u < 7; ++u) m = max(m, max(pr[u].y, pr[u].w));
      if (!__any(m == 0xffffffffu) || spins > (1u << 16)) break;
      asm volatile("s_sleep 2" ::: "memory");
    }
    STAMP(5);
#pragma unroll
    for (int u = 0; u < 7; ++u) {
      const int U = 7 * Cg + u;
      u2 pq[2];
#pragma unroll
      for (int q = 0; q < 2; ++q) {
        const unsigned pa = pr[u][2 * q], pb = pr[u][2 * q + 1];
        const f4 gg = acc[u][q] + (f4){bflo(pa), bfhi(pa), bflo(pb), bfhi(pb)} + __uint_as_float(bsr[q] << 16);
        const unsigned za = z1[u][2 * q], zb = z1[u][2 * q + 1];
        pq[q] = (u2){pkbf(bflo(za) * gg.x, bfhi(za) * gg.y), pkbf(bflo(zb) * gg.z, bfhi(zb) * gg.w)}; }
      u4 t = pair16(pq[0], pq[1]); t.y = min(t.y, 0xfffffffeu); t.w = min(t.w, 0xfffffffeu);
      if (U < 54) st16(MKB(ws + WS_P, 128 * 27 * 1024), (g & 1) * 27 * 1024 + ((g >> 1) * 16 + c) * 16, ((8 * PC + 2 * w) * 27 + (U >> 1)) * 1024 + (U & 1) * 512, t);
    }
  }
  STAMP(4); STAMP_OUT(ws, 2); }
  if constexpr (P1 == 2) return;
  { STAMP_DECL STAMP(0);
  const rsrc_t_b rP = MKB(ws + WS_P, 128 * 27 * 1024);
  BAR();
  STAMP(1);
  asm volatile("s_sleep %0" ::"n"(D_XS) : "memory");
  const int vp = (4 * tp + w) * 27 * 1024 + lane * 16;
  u4 fa[27];
#pragma unroll
  for (int j = 0; j < 27; ++j) fa[j] = __builtin_amdgcn_raw_buffer_load_b128(rP, vp, j * 1024, 16);
  f4 acc[5];
#pragma unroll
  for (int u = 0; u < 5; ++u) acc[u] = (f4){0, 0, 0, 0};
  b8 fw[2][5];
  auto rd = [&](int j) {
#pragma unroll
    for (int u = 0; u < 5; ++u) fw[j & 1][u] = *(LAS b8*)(LP(lane * 16 + (j >= 12 ? 61440 : 0) + (j >= 24 ? 61440 : 0)) + (5 * (j % 12) + u) * 1024); };
  rd(0);
#pragma unroll
  for (int j = 0; j < 27; ++j) {
    __builtin_amdgcn_sched_barrier(0);
    if (__builtin_expect(__any(max(fa[j].y, fa[j].w) == 0xffffffffu), 0)) {
      unsigned spins = 0;
      do { int v2 = vp; asm volatile("s_sleep 1" : "+v"(v2) :: "memory");
        fa[j] = __builtin_amdgcn_raw_buffer_load_b128(rP, v2, j * 1024, 16);
      } while (__any(max(fa[j].y, fa[j].w) == 0xffffffffu) && ++spins < (1u << 16)); }
    if (j < 26) rd(j + 1);
#pragma unroll
    for (int u = 0; u < 5; ++u) acc[u] = MFMA(fw[j & 1][u], __builtin_bit_cast(b8, fa[j]), acc[u]);
  }
  __builtin_amdgcn_sched_barrier(0);
  STAMP(2);
  us* o = out + (size_t)(64 * tp + 16 * w + c) * D_ + 80 * dg + 4 * g;
#pragma unroll
  for (int u = 0; u < 5; ++u) {
    const u2 h = __builtin_bit_cast(u2, __builtin_amdgcn_ds_read_tr16_b64_v4i16((LAS s4*)(S + D_H + (16 * u + 4 * g + ((lane >> 2) & 3)) * 128 + 32 * w + 8 * (lane & 3))));
    *(u2*)(o + 16 * u) = (u2){pkbf(bflo(h.x) + acc[u].x, bfhi(h.x) + acc[u].y), pkbf(bflo(h.y) + acc[u].z, bfhi(h.y) + acc[u].w)};
  }
  STAMP(3); STAMP_OUT(ws, 3); }
}
#define ARGS const us* __restrict__ x, const us* __restrict__ lar, const us* __restrict__ aim, const us* __restrict__ cre, const us* __restrict__ cim, const us* __restrict__ ldt, \
  const us* __restrict__ dsk, const us* __restrict__ win, const us* __restrict__ wsp, const us* __restrict__ bsp, const us* __restrict__ wout, us* __restrict__ out, char* __restrict__ ws
#define KERN(name, p0, p1) extern "C" __global__ __launch_bounds__(512, 2) void name(ARGS) { body<p0, p1>(x, lar, aim, cre, cim, ldt, dsk, win, wsp, bsp, wout, out, ws); }
#define LAUNCH(name) (void)hipFuncSetAttribute((const void*)name, hipFuncAttributeMaxDynamicSharedMemorySize, F_LDS); \
  name<<<256, 512, F_LDS, stream>>>((const us*)d_in[0], (const us*)d_in[1], (const us*)d_in[2], (const us*)d_in[3], (const us*)d_in[4], (const us*)d_in[5], (const us*)d_in[6], \
                                  (const us*)d_in[8], (const us*)d_in[9], (const us*)d_in[10], (const us*)d_in[11], (us*)d_out, (char*)d_ws);
KERN(kF, 0, 0) KERN(k1, 1, 1) KERN(k2, 2, 3)
#define LAUNCHES LAUNCH(kF) LAUNCH(k1) LAUNCH(k2)
extern "C" void kernel_launch(void* const* d_in, const int* in_sizes, int n_in, void* d_out, int out_size, void* d_ws, size_t ws_size, hipStream_t stream) {
  LAUNCHES
}
```
